```python
import torch

def _softmax(x, axis=-1):
    return torch.softmax(x, dim=axis)
def _gelu(x):
    return torch.nn.functional.gelu(x, approximate="tanh")
def _silu(x):
    return torch.nn.functional.silu(x)
def _sigmoid(x):
    return torch.sigmoid(x)
def _layernorm(x, axis=-1, eps=1e-5):
    m = x.mean(dim=axis, keepdim=True)
    v = x.var(dim=axis, keepdim=True, unbiased=False)
    return (x - m) / torch.sqrt(v + eps)
def _scatter_add(tgt, idx, src, axis=0):
    return tgt.clone().index_add_(axis, idx, src)
def _segment_sum(src, seg, n):
    out = torch.zeros((n,) + src.shape[1:], dtype=src.dtype)
    return out.index_add_(0, seg, src)
def _pad(x, pw):
    flat = tuple(v for pair in reversed(pw) for v in pair)
    return torch.nn.functional.pad(x, flat)

kernel_name = 'standalone__deep_cross_pnn__recsys'

def setup_inputs():

    _w = (lambda a: (lambda t: t.to(torch.bfloat16) if t.is_floating_point() else t)(torch.as_tensor(a)))
    B, F, D, V = 4096, 100, 64, min(1000, 100000)
    H = 4 * D
    return {
        "idx": _w((lambda lo, hi, *s: torch.randint(lo, hi, s))(0, V, B, F).to(torch.int64)),
        "table": _w((lambda *s: torch.randn(*s, dtype=torch.bfloat16) * 1.6)(V, D) * (D ** -0.5)),
        "w_p": _w((lambda *s: torch.randn(*s, dtype=torch.bfloat16) * 0.05)(F * F) * ((F * F) ** -0.5)),
        "w1": _w((lambda *s: torch.randn(*s, dtype=torch.bfloat16) * 1.6)(F * D, H) * ((F * D) ** -0.5)),
        "b1": _w((lambda *s: torch.randn(*s, dtype=torch.bfloat16) * 1.6)(H) * 0.01),
        "w2": _w((lambda *s: torch.randn(*s, dtype=torch.bfloat16) * 0.05)(H) * (H ** -0.5)),
        "b2": _w((lambda *s: torch.randn(*s, dtype=torch.bfloat16) * 0.05)(1) * 0.01),
    }

def reference(idx, table, w_p, w1, b1, w2, b2):
    emb = table[idx]
    B = emb.shape[0]; F = emb.shape[1]; D = emb.shape[2]
    ip = torch.einsum('bid,bjd->bij', emb, emb)
    ip = ip.reshape(B, F * F)
    logit_p = ip @ w_p
    z = emb.reshape(B, F * D)
    h = _gelu(z @ w1 + b1)
    logit_d = h @ w2 + b2
    return logit_p + logit_d
```

```cpp
#include <hip/hip_runtime.h>
#include <hip/hip_bf16.h>
#include <stdint.h>

typedef __attribute__((ext_vector_type(8))) short bf16x8;
typedef __attribute__((ext_vector_type(4))) float f32x4;
typedef __attribute__((ext_vector_type(4))) unsigned u32x4;
typedef __attribute__((ext_vector_type(2))) unsigned u32x2;
typedef __attribute__((ext_vector_type(4), aligned(4))) int i32x4u;

#define GAS __attribute__((address_space(1)))
#define LAS __attribute__((address_space(3)))
#define RLX_AGENT __ATOMIC_RELAXED, __HIP_MEMORY_SCOPE_AGENT

constexpr int NF = 100;
constexpr int LDS_ZROW = 128000;
constexpr int LDS_SPARE = 128128;
constexpr int LDS_TOTAL = LDS_SPARE + 32768;

constexpr size_t WS_FLAGS = 0;
constexpr size_t WS_CNT = 65536;
constexpr size_t WS_ZERO_BYTES = 81920;
constexpr size_t WS_X = 131072;
constexpr size_t WS_PART = WS_X + 1024 * 4096;

__device__ __forceinline__ float bf2f(unsigned short u) { return __uint_as_float(((unsigned)u) << 16); }
__device__ __forceinline__ float bfround(float x) {
    return __bfloat162float(__float2bfloat16(x));
}
__device__ __forceinline__ float gelu_tanh(float x) {
    const float u = 0.7978845608028654f * (x + 0.044715f * x * x * x);
    const float e = __expf(2.0f * u);
    const float t = 1.0f - 2.0f * __builtin_amdgcn_rcpf(e + 1.0f);
    return 0.5f * x * (1.0f + t);
}

__device__ __forceinline__ bf16x8 lds_read128(const char* smem, unsigned off) {
    return *reinterpret_cast<const bf16x8*>(smem + off);
}

__global__ __launch_bounds__(256, 1)
void dcp_kernel(const int* __restrict__ idx, const unsigned short* __restrict__ table,
                const unsigned short* __restrict__ wp, const unsigned short* __restrict__ w1,
                const unsigned short* __restrict__ b1, const unsigned short* __restrict__ w2,
                const unsigned short* __restrict__ b2, __hip_bfloat16* __restrict__ out,
                char* __restrict__ ws)
{
    __shared__ __attribute__((aligned(16))) char smem[LDS_TOTAL];

    const int tid = threadIdx.x;
    const int lane = tid & 63;
    const int w = __builtin_amdgcn_readfirstlane(tid >> 6);
    const int bid = blockIdx.x;
    const int mt = bid >> 3, nt = (bid >> 1) & 3, kh = bid & 1;
    const int c = lane & 15, q = lane >> 4;
    const int odd = w & 1;

    {
        const int srow = lane >> 3, slot = lane & 7;
        for (int p = w; p < 125; p += 4) {
            const int row = p * 8 + srow;
            const int chunk = slot ^ ((row >> 1) & 7);
            __builtin_amdgcn_global_load_lds(
                (const GAS unsigned*)((const char*)table + row * 128 + chunk * 16),
                (LAS unsigned*)(smem + p * 1024), 16, 0, 0);
        }
        if (tid < 8) *reinterpret_cast<u32x4*>(smem + LDS_ZROW + tid * 16) = (u32x4){0u, 0u, 0u, 0u};
    }

    const int ks0 = kh * 100 + w * 25;
    const int f0 = ks0 >> 1;
    unsigned addr[8][13];
#pragma unroll
    for (int j = 0; j < 4; ++j) {
#pragma unroll
        for (int h = 0; h < 2; ++h) {
            const int o = (w + j) & 3;
            const int sample = mt * 128 + ((h ^ kh) * 64) + o * 16 + c;
            const int* ip = idx + sample * NF + f0;
            const i32x4u v0 = *reinterpret_cast<const i32x4u*>(ip);
            const i32x4u v1 = *reinterpret_cast<const i32x4u*>(ip + 4);
            const i32x4u v2 = *reinterpret_cast<const i32x4u*>(ip + 8);
            const int v3 = ip[12];
            int vv[13] = {v0[0], v0[1], v0[2], v0[3], v1[0], v1[1], v1[2], v1[3], v2[0], v2[1], v2[2], v2[3], v3};
#pragma unroll
            for (int fi = 0; fi < 13; ++fi) {
                const unsigned v = (unsigned)vv[fi];
                unsigned a = v * 128u + (((unsigned)q ^ ((v >> 1) & 7u)) << 4);
                asm volatile("" : "+v"(a));
                addr[j * 2 + h][fi] = a;
            }
        }
    }

    const char* w1lane = (const char*)w1 + ((size_t)(2 * f0) * 32 + 8 * q) * 512 + (nt * 64 + 4 * c) * 2;
    u32x2 braw[3][8];
#pragma unroll
    for (int s = 0; s < 3; ++s)
#pragma unroll
        for (int jj = 0; jj < 8; ++jj)
            braw[s][jj] = *reinterpret_cast<const u32x2*>(w1lane + s * 16384 + jj * 512);

    {
        int t = 0;
#pragma unroll
        for (int I = 0; I < 7; ++I) {
#pragma unroll
            for (int J = I; J < 7; ++J, ++t) {
                if ((t & 3) == w) {
                    const int jcol = 16 * J + c;
                    const int jc = jcol < NF ? jcol : NF - 1;
                    const int i0 = 16 * I + 4 * q;
                    const int i0c = i0 < NF ? i0 : NF - 4;
                    f32x4 cf;
                    u32x2 tr = (u32x2){0u, 0u};
                    if (I != J) tr = *reinterpret_cast<const u32x2*>(wp + jc * NF + i0c);
#pragma unroll
                    for (int r = 0; r < 4; ++r) {
                        float a = bf2f(wp[(i0c + r) * NF + jc]);
                        const unsigned tw = (r & 2) ? tr[1] : tr[0];
                        const float b = (r & 1) ? __uint_as_float(tw & 0xffff0000u) : __uint_as_float(tw << 16);
                        cf[r] = (i0 < NF && jcol < NF) ? (a + b) : 0.0f;
                    }
                    *reinterpret_cast<f32x4*>(smem + LDS_SPARE + t * 1024 + lane * 16) = cf;
                }
            }
        }
    }

    __syncthreads();

    f32x4 acc[8][4];
#pragma unroll
    for (int t = 0; t < 8; ++t)
#pragma unroll
        for (int u = 0; u < 4; ++u) acc[t][u] = (f32x4){0.f, 0.f, 0.f, 0.f};

    bf16x8 af[8];
#pragma unroll
    for (int t = 0; t < 8; ++t) af[t] = lds_read128(smem, addr[t][0]);

    u32x4 bc[4], bn[4];
#pragma unroll
    for (int p = 0; p < 4; ++p) {
        const u32x2 a = braw[0][2 * p], b = braw[0][2 * p + 1];
        bc[0][p] = __builtin_amdgcn_perm(b[0], a[0], 0x05040100u);
        bc[1][p] = __builtin_amdgcn_perm(b[0], a[0], 0x07060302u);
        bc[2][p] = __builtin_amdgcn_perm(b[1], a[1], 0x05040100u);
        bc[3][p] = __builtin_amdgcn_perm(b[1], a[1], 0x07060302u);
    }
#pragma unroll
    for (int jj = 0; jj < 8; ++jj)
        braw[0][jj] = *reinterpret_cast<const u32x2*>(w1lane + 3 * 16384 + jj * 512);
    __builtin_amdgcn_sched_barrier(0);

#pragma unroll
    for (int s = 0; s < 26; ++s) {
        const bool edge = (s == 0) || (s == 25);
        if (edge) {
            const bool active = (s == 0) ? (odd == 0) : (odd != 0);
            if (active) {
#pragma unroll
                for (int t = 0; t < 8; ++t)
#pragma unroll
                    for (int u = 0; u < 4; ++u)
                        acc[t][u] = __builtin_amdgcn_mfma_f32_16x16x32_bf16(af[t], __builtin_bit_cast(bf16x8, bc[u]), acc[t][u], 0, 0, 0);
            }
            __builtin_amdgcn_sched_barrier(0);
        }
#pragma unroll
        for (int t = 0; t < 8; ++t) {
            if (!edge) {
#pragma unroll
                for (int u = 0; u < 4; ++u)
                    acc[t][u] = __builtin_amdgcn_mfma_f32_16x16x32_bf16(af[t], __builtin_bit_cast(bf16x8, bc[u]), acc[t][u], 0, 0, 0);
            }
            if (s + 1 < 26) {
                af[t] = lds_read128(smem, addr[t][(s + 1) >> 1] ^ (((s + 1) & 1) ? 64u : 0u));
                const int p = t >> 1;
                const u32x2 a = braw[(s + 1) % 3][2 * p], b = braw[(s + 1) % 3][2 * p + 1];
                if ((t & 1) == 0) {
                    bn[0][p] = __builtin_amdgcn_perm(b[0], a[0], 0x05040100u);
                    bn[1][p] = __builtin_amdgcn_perm(b[0], a[0], 0x07060302u);
                } else {
                    bn[2][p] = __builtin_amdgcn_perm(b[1], a[1], 0x05040100u);
                    bn[3][p] = __builtin_amdgcn_perm(b[1], a[1], 0x07060302u);
                    if (s + 4 < 26) {
                        braw[(s + 1) % 3][2 * p] = *reinterpret_cast<const u32x2*>(w1lane + (s + 4) * 16384 + (2 * p) * 512);
                        braw[(s + 1) % 3][2 * p + 1] = *reinterpret_cast<const u32x2*>(w1lane + (s + 4) * 16384 + (2 * p + 1) * 512);
                    }
                }
            }
            if (!edge) {
                __builtin_amdgcn_sched_group_barrier(0x008, 4, 0);
                __builtin_amdgcn_sched_group_barrier(0x100, 1, 0);
                __builtin_amdgcn_sched_group_barrier(0x002, 3, 0);
                __builtin_amdgcn_sched_group_barrier(0x020, 2, 0);
            }
        }
        if (s + 1 < 26) {
#pragma unroll
            for (int u = 0; u < 4; ++u) { asm volatile("" : "+v"(bn[u])); bc[u] = bn[u]; }
        }
        __builtin_amdgcn_sched_barrier(0);
    }
    asm volatile("" ::: "memory");

    f32x4 cf[28];
#pragma unroll
    for (int t = 0; t < 28; ++t) {
        cf[t] = *reinterpret_cast<const f32x4*>(smem + LDS_SPARE + t * 1024 + lane * 16);
    }

    const int prow0 = mt * 128 + kh * 64 + w * 16;
    unsigned paddr[4][7];
#pragma unroll
    for (int i = 0; i < 4; ++i) {
        const int* ip = idx + (prow0 + nt * 4 + i) * NF;
#pragma unroll
        for (int I = 0; I < 7; ++I) {
            const int fld = (I < 6) ? 16 * I + c : (c < 4 ? 96 + c : 0);
            paddr[i][I] = (unsigned)ip[fld];
        }
    }
#pragma unroll
    for (int t = 0; t < 28; ++t) asm volatile("" : "+v"(cf[t]));

    __syncthreads();
#pragma unroll
    for (int r = 1; r < 4; ++r) {
        char* dst = smem + LDS_SPARE + (((w + r) & 3) * 8192) + lane * 16;
#pragma unroll
        for (int h = 0; h < 2; ++h)
#pragma unroll
            for (int u = 0; u < 4; ++u)
                *reinterpret_cast<f32x4*>(dst + (h * 4 + u) * 1024) = acc[r * 2 + h][u];
        __syncthreads();
        const char* src = smem + LDS_SPARE + w * 8192 + lane * 16;
#pragma unroll
        for (int h = 0; h < 2; ++h)
#pragma unroll
            for (int u = 0; u < 4; ++u)
                acc[h][u] += *reinterpret_cast<const f32x4*>(src + (h * 4 + u) * 1024);
        if (r < 3) __syncthreads();
    }

    const __amdgpu_buffer_rsrc_t xr = __builtin_amdgcn_make_buffer_rsrc(ws + WS_X, 0, 1024 * 4096 + 65536, 0x00020000);
    {
        const unsigned off = (unsigned)((bid * 4 + w) * 4096 + lane * 16);
#pragma unroll
        for (int u = 0; u < 4; ++u)
            __builtin_amdgcn_raw_buffer_store_b128(__builtin_bit_cast(u32x4, acc[1][u]), xr, off + u * 1024, 0, 16);
        asm volatile("s_waitcnt vmcnt(0)" ::: "memory");
        if (lane == 0)
            __hip_atomic_store((GAS unsigned*)(ws + WS_FLAGS) + (bid * 4 + w) * 16, 1u, RLX_AGENT);
    }

#pragma unroll
    for (int i = 0; i < 4; ++i)
#pragma unroll
        for (int I = 0; I < 7; ++I) {
            unsigned v = paddr[i][I];
            if (I == 6) v = (c < 4) ? v : 1000u;
            paddr[i][I] = v * 128u + (((unsigned)q ^ ((v >> 1) & 7u)) << 4);
        }
    float lp[4];
#pragma unroll
    for (int i = 0; i < 4; ++i) {
        bf16x8 e[7][2];
#pragma unroll
        for (int I = 0; I < 7; ++I) {
            e[I][0] = lds_read128(smem, paddr[i][I]);
            e[I][1] = lds_read128(smem, paddr[i][I] ^ 64u);
        }
#pragma unroll
        for (int I = 0; I < 7; ++I) { asm volatile("" : "+a"(e[I][0])); asm volatile("" : "+a"(e[I][1])); }
        float s0 = 0.f, s1 = 0.f, s2 = 0.f, s3 = 0.f;
        f32x4 d[3];
        int t = 0;
#pragma unroll
        for (int I = 0; I < 7; ++I) {
#pragma unroll
            for (int J = I; J < 7; ++J, ++t) {
                if (t < 2) {
                    asm volatile(
                        "s_nop 1\n\t"
                        "v_mfma_f32_16x16x32_bf16 %0, %1, %2, 0\n\t"
                        "v_mfma_f32_16x16x32_bf16 %0, %3, %4, %0"
                        : "=&v"(d[t % 3])
                        : "a"(e[I][0]), "a"(e[J][0]), "a"(e[I][1]), "a"(e[J][1]));
                } else {
                    const f32x4 dp = d[(t + 1) % 3];
                    const f32x4 cp = cf[t - 2];
                    if (t == 2) {
                        asm volatile(
                            "v_mfma_f32_16x16x32_bf16 %0, %5, %6, 0\n\t"
                            "s_nop 4\n\t"
                            "v_fmac_f32 %1, %9, %13\n\t"
                            "v_fmac_f32 %2, %10, %14\n\t"
                            "v_mfma_f32_16x16x32_bf16 %0, %7, %8, %0\n\t"
                            "v_fmac_f32 %3, %11, %15\n\t"
                            "v_fmac_f32 %4, %12, %16"
                            : "=&v"(d[t % 3]), "+v"(s0), "+v"(s1), "+v"(s2), "+v"(s3)
                            : "a"(e[I][0]), "a"(e[J][0]), "a"(e[I][1]), "a"(e[J][1]),
                              "v"(cp[0]), "v"(cp[1]), "v"(cp[2]), "v"(cp[3]),
                              "v"(dp[0]), "v"(dp[1]), "v"(dp[2]), "v"(dp[3]));
                    } else {
                        asm volatile(
                            "v_mfma_f32_16x16x32_bf16 %0, %5, %6, 0\n\t"
                            "v_fmac_f32 %1, %9, %13\n\t"
                            "v_fmac_f32 %2, %10, %14\n\t"
                            "v_mfma_f32_16x16x32_bf16 %0, %7, %8, %0\n\t"
                            "v_fmac_f32 %3, %11, %15\n\t"
                            "v_fmac_f32 %4, %12, %16"
                            : "=&v"(d[t % 3]), "+v"(s0), "+v"(s1), "+v"(s2), "+v"(s3)
                            : "a"(e[I][0]), "a"(e[J][0]), "a"(e[I][1]), "a"(e[J][1]),
                              "v"(cp[0]), "v"(cp[1]), "v"(cp[2]), "v"(cp[3]),
                              "v"(dp[0]), "v"(dp[1]), "v"(dp[2]), "v"(dp[3]));
                    }
                }
            }
        }
        {
            const f32x4 da = d[26 % 3], db = d[27 % 3], ca = cf[26], cb = cf[27];
            asm volatile(
                "s_nop 1\n\t"
                "v_fmac_f32 %0, %4, %8\n\t"
                "v_fmac_f32 %1, %5, %9\n\t"
                "v_fmac_f32 %2, %6, %10\n\t"
                "v_fmac_f32 %3, %7, %11\n\t"
                "v_fmac_f32 %0, %12, %16\n\t"
                "v_fmac_f32 %1, %13, %17\n\t"
                "v_fmac_f32 %2, %14, %18\n\t"
                "v_fmac_f32 %3, %15, %19"
                : "+v"(s0), "+v"(s1), "+v"(s2), "+v"(s3)
                : "v"(ca[0]), "v"(ca[1]), "v"(ca[2]), "v"(ca[3]), "v"(da[0]), "v"(da[1]), "v"(da[2]), "v"(da[3]),
                  "v"(cb[0]), "v"(cb[1]), "v"(cb[2]), "v"(cb[3]), "v"(db[0]), "v"(db[1]), "v"(db[2]), "v"(db[3]));
        }
        float s = (s0 + s1) + (s2 + s3);
#pragma unroll
        for (int m = 1; m < 64; m <<= 1) s += __shfl_xor(s, m, 64);
        lp[i] = s;
        __builtin_amdgcn_sched_barrier(0);
    }

    {
        GAS unsigned* fl = (GAS unsigned*)(ws + WS_FLAGS) + ((bid ^ 1) * 4 + w) * 16;
        unsigned spins = 0;
        while ((unsigned)__builtin_amdgcn_readfirstlane(__hip_atomic_load(fl, RLX_AGENT)) != 1u) {
            __builtin_amdgcn_s_sleep(1);
            if (++spins > (1u << 24)) break;
        }
        __builtin_amdgcn_fence(__ATOMIC_ACQUIRE, "agent");
        const unsigned off = (unsigned)(((bid ^ 1) * 4 + w) * 4096 + lane * 16);
#pragma unroll
        for (int u = 0; u < 4; ++u)
            acc[0][u] += __builtin_bit_cast(f32x4, __builtin_amdgcn_raw_buffer_load_b128(xr, off + u * 1024, 0, 16));
    }

    float rs[4];
    {
        const int col0 = nt * 64 + 4 * c;
        const u32x2 b1v = *reinterpret_cast<const u32x2*>(b1 + col0);
        const u32x2 w2v = *reinterpret_cast<const u32x2*>(w2 + col0);
        const float bb[4] = {__uint_as_float(b1v[0] << 16), __uint_as_float(b1v[0] & 0xffff0000u),
                             __uint_as_float(b1v[1] << 16), __uint_as_float(b1v[1] & 0xffff0000u)};
        const float ww[4] = {__uint_as_float(w2v[0] << 16), __uint_as_float(w2v[0] & 0xffff0000u),
                             __uint_as_float(w2v[1] << 16), __uint_as_float(w2v[1] & 0xffff0000u)};
#pragma unroll
        for (int r = 0; r < 4; ++r) {
            float a = 0.f;
#pragma unroll
            for (int u = 0; u < 4; ++u) {
                float x = bfround(acc[0][u][r]);
                x = bfround(x + bb[u]);
                const float hh = bfround(gelu_tanh(x));
                a = fmaf(hh, ww[u], a);
            }
#pragma unroll
            for (int m = 1; m < 16; m <<= 1) a += __shfl_xor(a, m, 64);
            rs[r] = a + ((q == nt) ? lp[r] : 0.f);
        }
    }

    {
        const int grp = (mt * 2 + kh) * 4 + w;
        if (c == 0) {
            const f32x4 v = (f32x4){rs[0], rs[1], rs[2], rs[3]};
            __builtin_amdgcn_raw_buffer_store_b128(__builtin_bit_cast(u32x4, v), xr,
                (unsigned)(1024 * 4096 + (grp * 4 + nt) * 64 + q * 16), 0, 16);
        }
        asm volatile("s_waitcnt vmcnt(0)" ::: "memory");
        unsigned old = 0;
        if (lane == 0)
            old = __hip_atomic_fetch_add((GAS unsigned*)(ws + WS_CNT) + grp * 16, 1u, RLX_AGENT);
        old = (unsigned)__builtin_amdgcn_readfirstlane(old);
        if (old == 3u) {
            __builtin_amdgcn_fence(__ATOMIC_ACQUIRE, "agent");
            if (lane < 16) {
                float s = 0.f;
#pragma unroll
                for (int k = 0; k < 4; ++k)
                    s += __uint_as_float(__builtin_amdgcn_raw_buffer_load_b32(xr,
                            (unsigned)(1024 * 4096 + (grp * 4 + k) * 64 + lane * 4), 0, 16));
                s += bf2f(b2[0]);
                out[prow0 + lane] = __float2bfloat16(s);
            }
        }
    }
}

extern "C" void kernel_launch(void* const* d_in, const int* in_sizes, int n_in,
                              void* d_out, int out_size, void* d_ws, size_t ws_size,
                              hipStream_t stream) {
    const int* idx = (const int*)d_in[0];
    const unsigned short* table = (const unsigned short*)d_in[1];
    const unsigned short* wp = (const unsigned short*)d_in[2];
    const unsigned short* w1 = (const unsigned short*)d_in[3];
    const unsigned short* b1 = (const unsigned short*)d_in[4];
    const unsigned short* w2 = (const unsigned short*)d_in[5];
    const unsigned short* b2 = (const unsigned short*)d_in[6];
    (void)hipMemsetAsync(d_ws, 0, WS_ZERO_BYTES, stream);
    dcp_kernel<<<256, 256, 0, stream>>>(idx, table, wp, w1, b1, w2, b2,
                                        (__hip_bfloat16*)d_out, (char*)d_ws);
}
```

```cpp
#include <hip/hip_runtime.h>
#include <hip/hip_bf16.h>
#include <stdint.h>

typedef __attribute__((ext_vector_type(8))) short bf16x8;
typedef __attribute__((ext_vector_type(4))) float f32x4;
typedef __attribute__((ext_vector_type(4))) unsigned u32x4;
typedef __attribute__((ext_vector_type(2))) unsigned u32x2;
typedef __attribute__((ext_vector_type(4), aligned(4))) int i32x4u;

#define GAS __attribute__((address_space(1)))
#define LAS __attribute__((address_space(3)))
#define RLX_AGENT __ATOMIC_RELAXED, __HIP_MEMORY_SCOPE_AGENT

constexpr int NF = 100;
constexpr int LDS_ZROW = 128000;
constexpr int LDS_SPARE = 128128;
constexpr int LDS_TOTAL = LDS_SPARE + 32768;

constexpr size_t WS_FLAGS = 0;
constexpr size_t WS_GRAN = 65536;
constexpr size_t WS_ZERO_BYTES = 65536 + 131072;
constexpr size_t WS_X = 262144;

#define STAMP(n) do {} while (0)
__device__ __forceinline__ float bf2f(unsigned short u) { return __uint_as_float(((unsigned)u) << 16); }
__device__ __forceinline__ float bfround(float x) {
    return __bfloat162float(__float2bfloat16(x));
}
__device__ __forceinline__ float gelu_tanh(float x) {
    const float u = 0.7978845608028654f * (x + 0.044715f * x * x * x);
    const float e = __expf(2.0f * u);
    const float t = 1.0f - 2.0f * __builtin_amdgcn_rcpf(e + 1.0f);
    return 0.5f * x * (1.0f + t);
}

__device__ __forceinline__ bf16x8 lds_read128(const char* smem, unsigned off) {
    return *reinterpret_cast<const bf16x8*>(smem + off);
}

__global__ __launch_bounds__(256, 1)
void dcp_kernel(const int* __restrict__ idx, const unsigned short* __restrict__ table,
                const unsigned short* __restrict__ wp, const unsigned short* __restrict__ w1,
                const unsigned short* __restrict__ b1, const unsigned short* __restrict__ w2,
                const unsigned short* __restrict__ b2, __hip_bfloat16* __restrict__ out,
                char* __restrict__ ws)
{
    __shared__ __attribute__((aligned(16))) char smem[LDS_TOTAL];

    const int tid = threadIdx.x;
    const int lane = tid & 63;
    const int w = __builtin_amdgcn_readfirstlane(tid >> 6);
    const int bid = blockIdx.x;
    const int mt = bid >> 3, nt = (bid >> 1) & 3, kh = bid & 1;
    const int c = lane & 15, q = lane >> 4;
    const int odd = w & 1;

    STAMP(0);
    {
        const int srow = lane >> 3, slot = lane & 7;
        for (int p = w; p < 125; p += 4) {
            const int row = p * 8 + srow;
            const int chunk = slot ^ ((row >> 1) & 7);
            __builtin_amdgcn_global_load_lds(
                (const GAS unsigned*)((const char*)table + row * 128 + chunk * 16),
                (LAS unsigned*)(smem + p * 1024), 16, 0, 0);
        }
        if (tid < 8) *reinterpret_cast<u32x4*>(smem + LDS_ZROW + tid * 16) = (u32x4){0u, 0u, 0u, 0u};
    }

    const int ks0 = kh * 100 + w * 25;
    const int f0 = ks0 >> 1;
    i32x4u iv[8][3]; int iv3[8];
#pragma unroll
    for (int j = 0; j < 4; ++j) {
#pragma unroll
        for (int h = 0; h < 2; ++h) {
            const int o = (w + j) & 3;
            const int sample = mt * 128 + ((h ^ kh) * 64) + o * 16 + c;
            const int* ip = idx + sample * NF + f0;
            iv[j * 2 + h][0] = *reinterpret_cast<const i32x4u*>(ip);
            iv[j * 2 + h][1] = *reinterpret_cast<const i32x4u*>(ip + 4);
            iv[j * 2 + h][2] = *reinterpret_cast<const i32x4u*>(ip + 8);
            iv3[j * 2 + h] = ip[12];
        }
    }
    const char* w1lane = (const char*)w1 + ((size_t)(2 * f0) * 32 + 8 * q) * 512 + (nt * 64 + 4 * c) * 2;
    u32x2 braw[3][8];
#pragma unroll
    for (int s = 0; s < 3; ++s)
#pragma unroll
        for (int jj = 0; jj < 8; ++jj)
            braw[s][jj] = *reinterpret_cast<const u32x2*>(w1lane + s * 16384 + jj * 512);
    unsigned short wa[7][4]; u32x2 wt[7]; bool wok[7], wdiag[7];
#pragma unroll
    for (int k = 0; k < 7; ++k) {
        const int t = w + 4 * k;
        const unsigned long long Ilo = 0x2221111110000000ull, Ihi = 0x0000655444333322ull;
        const unsigned long long Jlo = 0x4326543216543210ull, Jhi = 0x0000665654654365ull;
        const int I = (int)(((t < 16 ? Ilo : Ihi) >> ((t & 15) * 4)) & 15);
        const int J = (int)(((t < 16 ? Jlo : Jhi) >> ((t & 15) * 4)) & 15);
        const int jcol = 16 * J + c;
        const int jc = jcol < NF ? jcol : NF - 1;
        const int i0 = 16 * I + 4 * q;
        const int i0c = i0 < NF ? i0 : NF - 4;
        wok[k] = (i0 < NF) && (jcol < NF);
        wdiag[k] = (I == J);
        wt[k] = *reinterpret_cast<const u32x2*>(wp + jc * NF + i0c);
#pragma unroll
        for (int r = 0; r < 4; ++r) wa[k][r] = wp[(i0c + r) * NF + jc];
    }
    __builtin_amdgcn_sched_barrier(0);

    unsigned addr[8][13];
#pragma unroll
    for (int t = 0; t < 8; ++t) {
#pragma unroll
        for (int fi = 0; fi < 13; ++fi) {
            const unsigned v = (unsigned)(fi < 12 ? iv[t][fi >> 2][fi & 3] : iv3[t]);
            unsigned a = v * 128u + (((unsigned)q ^ ((v >> 1) & 7u)) << 4);
            asm volatile("" : "+v"(a));
            addr[t][fi] = a;
        }
    }
#pragma unroll
    for (int k = 0; k < 7; ++k) {
        f32x4 cfv;
#pragma unroll
        for (int r = 0; r < 4; ++r) {
            const float a = bf2f(wa[k][r]);
            const unsigned tw = (r & 2) ? wt[k][1] : wt[k][0];
            const float b = (r & 1) ? __uint_as_float(tw & 0xffff0000u) : __uint_as_float(tw << 16);
            cfv[r] = wok[k] ? (wdiag[k] ? a : a + b) : 0.0f;
        }
        *reinterpret_cast<f32x4*>(smem + LDS_SPARE + (w + 4 * k) * 1024 + lane * 16) = cfv;
    }

    STAMP(1);
    __syncthreads();
    STAMP(2);

    f32x4 acc[8][4];
#pragma unroll
    for (int t = 0; t < 8; ++t)
#pragma unroll
        for (int u = 0; u < 4; ++u) acc[t][u] = (f32x4){0.f, 0.f, 0.f, 0.f};

    bf16x8 af[8];
#pragma unroll
    for (int t = 0; t < 8; ++t) af[t] = lds_read128(smem, addr[t][0]);

    u32x4 bc[4], bn[4];
#pragma unroll
    for (int p = 0; p < 4; ++p) {
        const u32x2 a = braw[0][2 * p], b = braw[0][2 * p + 1];
        bc[0][p] = __builtin_amdgcn_perm(b[0], a[0], 0x05040100u);
        bc[1][p] = __builtin_amdgcn_perm(b[0], a[0], 0x07060302u);
        bc[2][p] = __builtin_amdgcn_perm(b[1], a[1], 0x05040100u);
        bc[3][p] = __builtin_amdgcn_perm(b[1], a[1], 0x07060302u);
    }
#pragma unroll
    for (int jj = 0; jj < 8; ++jj)
        braw[0][jj] = *reinterpret_cast<const u32x2*>(w1lane + 3 * 16384 + jj * 512);
    __builtin_amdgcn_sched_barrier(0);

#pragma unroll
    for (int s = 0; s < 26; ++s) {
        const bool edge = (s == 0) || (s == 25);
        if (edge) {
            const bool active = (s == 0) ? (odd == 0) : (odd != 0);
            if (active) {
#pragma unroll
                for (int t = 0; t < 8; ++t)
#pragma unroll
                    for (int u = 0; u < 4; ++u)
                        acc[t][u] = __builtin_amdgcn_mfma_f32_16x16x32_bf16(af[t], __builtin_bit_cast(bf16x8, bc[u]), acc[t][u], 0, 0, 0);
            }
            __builtin_amdgcn_sched_barrier(0);
        }
#pragma unroll
        for (int t = 0; t < 8; ++t) {
            if (!edge) {
#pragma unroll
                for (int u = 0; u < 4; ++u)
                    acc[t][u] = __builtin_amdgcn_mfma_f32_16x16x32_bf16(af[t], __builtin_bit_cast(bf16x8, bc[u]), acc[t][u], 0, 0, 0);
            }
            if (s + 1 < 26) {
                af[t] = lds_read128(smem, addr[t][(s + 1) >> 1] ^ (((s + 1) & 1) ? 64u : 0u));
                const int p = t >> 1;
                const u32x2 a = braw[(s + 1) % 3][2 * p], b = braw[(s + 1) % 3][2 * p + 1];
                if ((t & 1) == 0) {
                    bn[0][p] = __builtin_amdgcn_perm(b[0], a[0], 0x05040100u);
                    bn[1][p] = __builtin_amdgcn_perm(b[0], a[0], 0x07060302u);
                } else {
                    bn[2][p] = __builtin_amdgcn_perm(b[1], a[1], 0x05040100u);
                    bn[3][p] = __builtin_amdgcn_perm(b[1], a[1], 0x07060302u);
                    if (s + 4 < 26) {
                        braw[(s + 1) % 3][2 * p] = *reinterpret_cast<const u32x2*>(w1lane + (s + 4) * 16384 + (2 * p) * 512);
                        braw[(s + 1) % 3][2 * p + 1] = *reinterpret_cast<const u32x2*>(w1lane + (s + 4) * 16384 + (2 * p + 1) * 512);
                    }
                }
            }
            if (!edge) {
                __builtin_amdgcn_sched_group_barrier(0x008, 4, 0);
                __builtin_amdgcn_sched_group_barrier(0x100, 1, 0);
                __builtin_amdgcn_sched_group_barrier(0x002, 3, 0);
                __builtin_amdgcn_sched_group_barrier(0x020, 2, 0);
            }
        }
        if (s + 1 < 26) {
#pragma unroll
            for (int u = 0; u < 4; ++u) { asm volatile("" : "+v"(bn[u])); bc[u] = bn[u]; }
        }
        __builtin_amdgcn_sched_barrier(0);
    }
    asm volatile("" ::: "memory");

    STAMP(3);
    f32x4 cf[28];
#pragma unroll
    for (int t = 0; t < 28; ++t) {
        cf[t] = *reinterpret_cast<const f32x4*>(smem + LDS_SPARE + t * 1024 + lane * 16);
    }

    const int prow0 = mt * 128 + kh * 64 + w * 16;
    unsigned paddr[4][7];
#pragma unroll
    for (int i = 0; i < 4; ++i) {
        const int* ip = idx + (prow0 + nt * 4 + i) * NF;
#pragma unroll
        for (int I = 0; I < 7; ++I) {
            const int fld = (I < 6) ? 16 * I + c : (c < 4 ? 96 + c : 0);
            paddr[i][I] = (unsigned)ip[fld];
        }
    }
#pragma unroll
    for (int t = 0; t < 28; ++t) asm volatile("" : "+v"(cf[t]));

    __syncthreads();
#pragma unroll
    for (int r = 1; r < 4; ++r) {
        char* dst = smem + LDS_SPARE + (((w + r) & 3) * 8192) + lane * 16;
#pragma unroll
        for (int h = 0; h < 2; ++h)
#pragma unroll
            for (int u = 0; u < 4; ++u)
                *reinterpret_cast<f32x4*>(dst + (h * 4 + u) * 1024) = acc[r * 2 + h][u];
        __syncthreads();
        const char* src = smem + LDS_SPARE + w * 8192 + lane * 16;
#pragma unroll
        for (int h = 0; h < 2; ++h)
#pragma unroll
            for (int u = 0; u < 4; ++u)
                acc[h][u] += *reinterpret_cast<const f32x4*>(src + (h * 4 + u) * 1024);
        if (r < 3) __syncthreads();
    }

    STAMP(4);
    const __amdgpu_buffer_rsrc_t xr = __builtin_amdgcn_make_buffer_rsrc(ws + WS_X, 0, 1024 * 4096, 0x00020000);
    {
        const unsigned off = (unsigned)((bid * 4 + w) * 4096 + lane * 16);
#pragma unroll
        for (int u = 0; u < 4; ++u)
            __builtin_amdgcn_raw_buffer_store_b128(__builtin_bit_cast(u32x4, acc[1][u]), xr, off + u * 1024, 0, 16);
    }

    STAMP(5);
#pragma unroll
    for (int i = 0; i < 4; ++i)
#pragma unroll
        for (int I = 0; I < 7; ++I) {
            unsigned v = paddr[i][I];
            if (I == 6) v = (c < 4) ? v : 1000u;
            paddr[i][I] = v * 128u + (((unsigned)q ^ ((v >> 1) & 7u)) << 4);
        }
    float lp[4];
    bf16x8 e[2][7][2];
#pragma unroll
    for (int I = 0; I < 7; ++I) {
        e[0][I][0] = lds_read128(smem, paddr[0][I]);
        e[0][I][1] = lds_read128(smem, paddr[0][I] ^ 64u);
    }
#pragma unroll
    for (int i = 0; i < 4; ++i) {
#pragma unroll
        for (int I = 0; I < 7; ++I) { asm volatile("" : "+a"(e[i & 1][I][0])); asm volatile("" : "+a"(e[i & 1][I][1])); }
        if (i < 3) {
#pragma unroll
            for (int I = 0; I < 7; ++I) {
                e[(i + 1) & 1][I][0] = lds_read128(smem, paddr[i + 1][I]);
                e[(i + 1) & 1][I][1] = lds_read128(smem, paddr[i + 1][I] ^ 64u);
            }
        }
        if (i == 1) {
            asm volatile("s_waitcnt vmcnt(0)" ::: "memory");
            if (lane == 0)
                __hip_atomic_store((GAS unsigned*)(ws + WS_FLAGS) + (bid * 4 + w) * 16, 1u, RLX_AGENT);
        }
        __builtin_amdgcn_sched_barrier(0);
        float s0 = 0.f, s1 = 0.f, s2 = 0.f, s3 = 0.f;
        f32x4 d[3];
        int t = 0;
#pragma unroll
        for (int I = 0; I < 7; ++I) {
#pragma unroll
            for (int J = I; J < 7; ++J, ++t) {
                if (t < 2) {
                    asm volatile(
                        "s_nop 1\n\t"
                        "v_mfma_f32_16x16x32_bf16 %0, %1, %2, 0\n\t"
                        "v_mfma_f32_16x16x32_bf16 %0, %3, %4, %0"
                        : "=&v"(d[t % 3])
                        : "a"(e[i & 1][I][0]), "a"(e[i & 1][J][0]), "a"(e[i & 1][I][1]), "a"(e[i & 1][J][1]));
                } else {
                    const f32x4 dp = d[(t + 1) % 3];
                    const f32x4 cp = cf[t - 2];
                    if (t == 2) {
                        asm volatile(
                            "v_mfma_f32_16x16x32_bf16 %0, %5, %6, 0\n\t"
                            "s_nop 4\n\t"
                            "v_fmac_f32 %1, %9, %13\n\t"
                            "v_fmac_f32 %2, %10, %14\n\t"
                            "v_mfma_f32_16x16x32_bf16 %0, %7, %8, %0\n\t"
                            "v_fmac_f32 %3, %11, %15\n\t"
                            "v_fmac_f32 %4, %12, %16"
                            : "=&v"(d[t % 3]), "+v"(s0), "+v"(s1), "+v"(s2), "+v"(s3)
                            : "a"(e[i & 1][I][0]), "a"(e[i & 1][J][0]), "a"(e[i & 1][I][1]), "a"(e[i & 1][J][1]),
                              "v"(cp[0]), "v"(cp[1]), "v"(cp[2]), "v"(cp[3]),
                              "v"(dp[0]), "v"(dp[1]), "v"(dp[2]), "v"(dp[3]));
                    } else {
                        asm volatile(
                            "v_mfma_f32_16x16x32_bf16 %0, %5, %6, 0\n\t"
                            "v_fmac_f32 %1, %9, %13\n\t"
                            "v_fmac_f32 %2, %10, %14\n\t"
                            "v_mfma_f32_16x16x32_bf16 %0, %7, %8, %0\n\t"
                            "v_fmac_f32 %3, %11, %15\n\t"
                            "v_fmac_f32 %4, %12, %16"
                            : "=&v"(d[t % 3]), "+v"(s0), "+v"(s1), "+v"(s2), "+v"(s3)
                            : "a"(e[i & 1][I][0]), "a"(e[i & 1][J][0]), "a"(e[i & 1][I][1]), "a"(e[i & 1][J][1]),
                              "v"(cp[0]), "v"(cp[1]), "v"(cp[2]), "v"(cp[3]),
                              "v"(dp[0]), "v"(dp[1]), "v"(dp[2]), "v"(dp[3]));
                    }
                }
            }
        }
        {
            const f32x4 da = d[26 % 3], db = d[27 % 3], ca = cf[26], cb = cf[27];
            asm volatile(
                "s_nop 1\n\t"
                "v_fmac_f32 %0, %4, %8\n\t"
                "v_fmac_f32 %1, %5, %9\n\t"
                "v_fmac_f32 %2, %6, %10\n\t"
                "v_fmac_f32 %3, %7, %11\n\t"
                "v_fmac_f32 %0, %12, %16\n\t"
                "v_fmac_f32 %1, %13, %17\n\t"
                "v_fmac_f32 %2, %14, %18\n\t"
                "v_fmac_f32 %3, %15, %19"
                : "+v"(s0), "+v"(s1), "+v"(s2), "+v"(s3)
                : "v"(ca[0]), "v"(ca[1]), "v"(ca[2]), "v"(ca[3]), "v"(da[0]), "v"(da[1]), "v"(da[2]), "v"(da[3]),
                  "v"(cb[0]), "v"(cb[1]), "v"(cb[2]), "v"(cb[3]), "v"(db[0]), "v"(db[1]), "v"(db[2]), "v"(db[3]));
        }
        lp[i] = (s0 + s1) + (s2 + s3);
        __builtin_amdgcn_sched_barrier(0);
    }
#pragma unroll
    for (int m = 1; m < 64; m <<= 1)
#pragma unroll
        for (int i = 0; i < 4; ++i) lp[i] += __shfl_xor(lp[i], m, 64);

    STAMP(6);
    {
        GAS unsigned* fl = (GAS unsigned*)(ws + WS_FLAGS) + ((bid ^ 1) * 4 + w) * 16;
        unsigned spins = 0;
        while ((unsigned)__builtin_amdgcn_readfirstlane(__hip_atomic_load(fl, RLX_AGENT)) != 1u) {
            __builtin_amdgcn_s_sleep(1);
            if (++spins > (1u << 24)) break;
        }
        __builtin_amdgcn_fence(__ATOMIC_ACQUIRE, "agent");
        const unsigned off = (unsigned)(((bid ^ 1) * 4 + w) * 4096 + lane * 16);
#pragma unroll
        for (int u = 0; u < 4; ++u)
            acc[0][u] += __builtin_bit_cast(f32x4, __builtin_amdgcn_raw_buffer_load_b128(xr, off + u * 1024, 0, 16));
    }

    STAMP(7);
    float rs[4];
    {
        const int col0 = nt * 64 + 4 * c;
        const u32x2 b1v = *reinterpret_cast<const u32x2*>(b1 + col0);
        const u32x2 w2v = *reinterpret_cast<const u32x2*>(w2 + col0);
        const float bb[4] = {__uint_as_float(b1v[0] << 16), __uint_as_float(b1v[0] & 0xffff0000u),
                             __uint_as_float(b1v[1] << 16), __uint_as_float(b1v[1] & 0xffff0000u)};
        const float ww[4] = {__uint_as_float(w2v[0] << 16), __uint_as_float(w2v[0] & 0xffff0000u),
                             __uint_as_float(w2v[1] << 16), __uint_as_float(w2v[1] & 0xffff0000u)};
#pragma unroll
        for (int r = 0; r < 4; ++r) {
            float a = 0.f;
#pragma unroll
            for (int u = 0; u < 4; ++u) {
                float x = bfround(acc[0][u][r]);
                x = bfround(x + bb[u]);
                const float hh = bfround(gelu_tanh(x));
                a = fmaf(hh, ww[u], a);
            }
#pragma unroll
            for (int m = 1; m < 16; m <<= 1) a += __shfl_xor(a, m, 64);
            rs[r] = a + ((q == nt) ? lp[r] : 0.f);
        }
    }

    {
        const int grp = (mt * 2 + kh) * 4 + w;
        GAS unsigned long long* g = (GAS unsigned long long*)(ws + WS_GRAN) + grp * 64;
        if (c == 0) {
#pragma unroll
            for (int r = 0; r < 4; ++r)
                __hip_atomic_store(g + nt * 16 + q * 4 + r, (1ull << 32) | (unsigned long long)__float_as_uint(rs[r]), RLX_AGENT);
        }
        float v = 0.f;
        unsigned spins = 0;
        for (;;) {
            bool ok = true;
            if (lane < 16) {
                const unsigned long long x = __hip_atomic_load(g + (lane >> 2) * 16 + nt * 4 + (lane & 3), RLX_AGENT);
                v = __uint_as_float((unsigned)x);
                ok = (x >> 32) == 1ull;
            }
            if (__all(ok)) break;
            __builtin_amdgcn_s_sleep(1);
            if (++spins > (1u << 24)) break;
        }
        v += __shfl_xor(v, 4, 64);
        v += __shfl_xor(v, 8, 64);
        if (lane < 4) out[prow0 + nt * 4 + lane] = __float2bfloat16(v + bf2f(b2[0]));
    }
    STAMP(8);
}

extern "C" void kernel_launch(void* const* d_in, const int* in_sizes, int n_in,
                              void* d_out, int out_size, void* d_ws, size_t ws_size,
                              hipStream_t stream) {
    const int* idx = (const int*)d_in[0];
    const unsigned short* table = (const unsigned short*)d_in[1];
    const unsigned short* wp = (const unsigned short*)d_in[2];
    const unsigned short* w1 = (const unsigned short*)d_in[3];
    const unsigned short* b1 = (const unsigned short*)d_in[4];
    const unsigned short* w2 = (const unsigned short*)d_in[5];
    const unsigned short* b2 = (const unsigned short*)d_in[6];
    (void)hipMemsetAsync(d_ws, 0, WS_ZERO_BYTES, stream);
    dcp_kernel<<<256, 256, 0, stream>>>(idx, table, wp, w1, b1, w2, b2,
                                        (__hip_bfloat16*)d_out, (char*)d_ws);
}
```

```cpp
#include <hip/hip_runtime.h>
#include <hip/hip_bf16.h>
#include <stdint.h>

typedef __attribute__((ext_vector_type(8))) short bf16x8;
typedef __attribute__((ext_vector_type(4))) float f32x4;
typedef __attribute__((ext_vector_type(4))) unsigned u32x4;
typedef __attribute__((ext_vector_type(2))) unsigned u32x2;
typedef __attribute__((ext_vector_type(4), aligned(4))) int i32x4u;

#define GAS __attribute__((address_space(1)))
#define LAS __attribute__((address_space(3)))
#define RLX_AGENT __ATOMIC_RELAXED, __HIP_MEMORY_SCOPE_AGENT

constexpr int NF = 100;
constexpr int LDS_ZROW = 128000;
constexpr int LDS_SPARE = 128128;
constexpr int LDS_RED = LDS_SPARE + 32768;
constexpr int LDS_TOTAL = LDS_RED + 256;

constexpr size_t WS_FLAGS = 0;
constexpr size_t WS_COEF = 4096;
constexpr size_t WS_W1T = 65536;
constexpr size_t WS_X = WS_W1T + 200 * 16384;

#define RSTAMP(n) do {} while (0)
#define STAMP(n) do {} while (0)

__device__ __forceinline__ float bf2f(unsigned short u) { return __uint_as_float(((unsigned)u) << 16); }
__device__ __forceinline__ float bfround(float x) {
    return __bfloat162float(__float2bfloat16(x));
}
__device__ __forceinline__ float gelu_tanh(float x) {
    const float u = 0.7978845608028654f * (x + 0.044715f * x * x * x);
    const float e = __expf(2.0f * u);
    const float t = 1.0f - 2.0f * __builtin_amdgcn_rcpf(e + 1.0f);
    return 0.5f * x * (1.0f + t);
}
__device__ __forceinline__ bf16x8 lds_read128(const char* smem, unsigned off) {
    return *reinterpret_cast<const bf16x8*>(smem + off);
}

__global__ __launch_bounds__(256)
void dcp_prep(const unsigned short* __restrict__ wp, const unsigned short* __restrict__ w1,
              char* __restrict__ ws)
{
    const int lane = threadIdx.x & 63;
    const int gw = __builtin_amdgcn_readfirstlane(blockIdx.x * 4 + (threadIdx.x >> 6));
    const int c = lane & 15, q = lane >> 4;
    if (gw < 800) {
        const int ks = gw >> 2, nt = gw & 3;
        const char* src = (const char*)w1 + ((size_t)ks * 32 + 8 * q) * 512 + (nt * 64 + 4 * c) * 2;
        u32x2 r[8];
#pragma unroll
        for (int jj = 0; jj < 8; ++jj) r[jj] = *reinterpret_cast<const u32x2*>(src + jj * 512);
        u32x4 p0, p1, p2, p3;
#pragma unroll
        for (int p = 0; p < 4; ++p) {
            const u32x2 a = r[2 * p], b = r[2 * p + 1];
            p0[p] = __builtin_amdgcn_perm(b[0], a[0], 0x05040100u);
            p1[p] = __builtin_amdgcn_perm(b[0], a[0], 0x07060302u);
            p2[p] = __builtin_amdgcn_perm(b[1], a[1], 0x05040100u);
            p3[p] = __builtin_amdgcn_perm(b[1], a[1], 0x07060302u);
        }
        char* dst = ws + WS_W1T + (size_t)gw * 4096 + lane * 16;
        *reinterpret_cast<u32x4*>(dst) = p0;
        *reinterpret_cast<u32x4*>(dst + 1024) = p1;
        *reinterpret_cast<u32x4*>(dst + 2048) = p2;
        *reinterpret_cast<u32x4*>(dst + 3072) = p3;
    } else if (gw < 828) {
        const int t = gw - 800;
        const unsigned long long Ilo = 0x2221111110000000ull, Ihi = 0x0000655444333322ull;
        const unsigned long long Jlo = 0x4326543216543210ull, Jhi = 0x0000665654654365ull;
        const int I = (int)(((t < 16 ? Ilo : Ihi) >> ((t & 15) * 4)) & 15);
        const int J = (int)(((t < 16 ? Jlo : Jhi) >> ((t & 15) * 4)) & 15);
        const int jcol = 16 * J + c;
        f32x4 cfv;
#pragma unroll
        for (int r = 0; r < 4; ++r) {
            const int i = 16 * I + 4 * q + r;
            float v = 0.f;
            if (i < NF && jcol < NF) {
                v = bf2f(wp[i * NF + jcol]);
                if (I != J) v += bf2f(wp[jcol * NF + i]);
            }
            cfv[r] = v;
        }
        *reinterpret_cast<f32x4*>(ws + WS_COEF + t * 1024 + lane * 16) = cfv;
    } else if (gw == 828) {
#pragma unroll
        for (int k = 0; k < 4; ++k)
            *reinterpret_cast<u32x4*>(ws + WS_FLAGS + k * 1024 + lane * 16) = (u32x4){0u, 0u, 0u, 0u};
    }
}

__global__ __launch_bounds__(256, 1)
void dcp_kernel(const int* __restrict__ idx, const unsigned short* __restrict__ table,
                const unsigned short* __restrict__ b1, const unsigned short* __restrict__ w2,
                const unsigned short* __restrict__ b2, __hip_bfloat16* __restrict__ out,
                char* __restrict__ ws)
{
    __shared__ __attribute__((aligned(16))) char smem[LDS_TOTAL];

    const int tid = threadIdx.x;
    const int lane = tid & 63;
    const int w = __builtin_amdgcn_readfirstlane(tid >> 6);
    const int bid = blockIdx.x;
    const int mt = bid >> 3, c8 = bid & 7, nt = c8 >> 1, kh = bid & 1;
    const int c = lane & 15, q = lane >> 4;
    const int odd = w & 1;

    RSTAMP(10);
    STAMP(0);
    {
        const int srow = lane >> 3, slot = lane & 7;
        for (int p = w; p < 125; p += 4) {
            const int row = p * 8 + srow;
            const int chunk = slot ^ ((row >> 1) & 7);
            __builtin_amdgcn_global_load_lds(
                (const GAS unsigned*)((const char*)table + row * 128 + chunk * 16),
                (LAS unsigned*)(smem + p * 1024), 16, 0, 0);
        }
        if (tid < 8) *reinterpret_cast<u32x4*>(smem + LDS_ZROW + tid * 16) = (u32x4){0u, 0u, 0u, 0u};
    }

    const int ks0 = kh * 100 + w * 25;
    const int f0 = ks0 >> 1;
    i32x4u iv[8][3]; int iv3[8];
#pragma unroll
    for (int j = 0; j < 4; ++j) {
#pragma unroll
        for (int h = 0; h < 2; ++h) {
            const int o = (w + j) & 3;
            const int sample = mt * 128 + h * 64 + o * 16 + c;
            const int* ip = idx + sample * NF + f0;
            iv[j * 2 + h][0] = *reinterpret_cast<const i32x4u*>(ip);
            iv[j * 2 + h][1] = *reinterpret_cast<const i32x4u*>(ip + 4);
            iv[j * 2 + h][2] = *reinterpret_cast<const i32x4u*>(ip + 8);
            iv3[j * 2 + h] = ip[12];
        }
    }
    const char* w1lane = ws + WS_W1T + ((size_t)(2 * f0) * 4 + nt) * 4096 + lane * 16;
    bf16x8 bq[4][4];
#pragma unroll
    for (int s = 0; s < 4; ++s)
#pragma unroll
        for (int u = 0; u < 4; ++u)
            bq[s][u] = *reinterpret_cast<const bf16x8*>(w1lane + s * 16384 + u * 1024);
    __builtin_amdgcn_sched_barrier(0);

    unsigned addr[8][13];
#pragma unroll
    for (int t = 0; t < 8; ++t) {
#pragma unroll
        for (int fi = 0; fi < 13; ++fi) {
            const unsigned v = (unsigned)(fi < 12 ? iv[t][fi >> 2][fi & 3] : iv3[t]);
            unsigned a = v * 128u + (((unsigned)q ^ ((v >> 1) & 7u)) << 4);
            asm volatile("" : "+v"(a));
            addr[t][fi] = a;
        }
    }

    STAMP(1);
    __syncthreads();
    STAMP(2);

    f32x4 acc[8][4];
#pragma unroll
    for (int t = 0; t < 8; ++t)
#pragma unroll
        for (int u = 0; u < 4; ++u) acc[t][u] = (f32x4){0.f, 0.f, 0.f, 0.f};

    bf16x8 af[8];
#pragma unroll
    for (int t = 0; t < 8; ++t) af[t] = lds_read128(smem, addr[t][0]);
    __builtin_amdgcn_sched_barrier(0);

#pragma unroll
    for (int s = 0; s < 26; ++s) {
        const bool edge = (s == 0) || (s == 25);
        if (edge) {
            const bool active = (s == 0) ? (odd == 0) : (odd != 0);
            if (active) {
#pragma unroll
                for (int t = 0; t < 8; ++t)
#pragma unroll
                    for (int u = 0; u < 4; ++u)
                        acc[t][u] = __builtin_amdgcn_mfma_f32_16x16x32_bf16(af[t], bq[s & 3][u], acc[t][u], 0, 0, 0);
            }
            __builtin_amdgcn_sched_barrier(0);
        }
#pragma unroll
        for (int t = 0; t < 8; ++t) {
            if (!edge) {
#pragma unroll
                for (int u = 0; u < 4; ++u)
                    acc[t][u] = __builtin_amdgcn_mfma_f32_16x16x32_bf16(af[t], bq[s & 3][u], acc[t][u], 0, 0, 0);
            }
            if (s + 1 < 26)
                af[t] = lds_read128(smem, addr[t][(s + 1) >> 1] ^ (((s + 1) & 1) ? 64u : 0u));
            if (s >= 1 && s + 3 < 26 && (t & 1) == 0) {
                bq[(s + 3) & 3][t >> 1] = *reinterpret_cast<const bf16x8*>(w1lane + (s + 3) * 16384 + (t >> 1) * 1024);
            }
            if (!edge) {
                __builtin_amdgcn_sched_group_barrier(0x008, 1, 0);
                __builtin_amdgcn_sched_group_barrier(0x002, 1, 0);
                __builtin_amdgcn_sched_group_barrier(0x008, 1, 0);
                __builtin_amdgcn_sched_group_barrier(0x020, 1, 0);
                __builtin_amdgcn_sched_group_barrier(0x008, 1, 0);
                __builtin_amdgcn_sched_group_barrier(0x002, 1, 0);
                __builtin_amdgcn_sched_group_barrier(0x008, 1, 0);
                __builtin_amdgcn_sched_group_barrier(0x100, 1, 0);
            }
        }
        if (s == 24) {
#pragma unroll
            for (int u = 0; u < 4; ++u) asm volatile("" : "+v"(bq[1][u]));
        }
        __builtin_amdgcn_sched_barrier(0);
    }
    asm volatile("" ::: "memory");

    STAMP(3);
    f32x4 cf[28];
#pragma unroll
    for (int t = 0; t < 28; ++t)
        cf[t] = *reinterpret_cast<const f32x4*>(ws + WS_COEF + t * 1024 + lane * 16);

    const int frow0 = mt * 128 + c8 * 16;
    unsigned paddr[4][7];
#pragma unroll
    for (int i = 0; i < 4; ++i) {
        const int* ip = idx + (frow0 + w * 4 + i) * NF;
#pragma unroll
        for (int I = 0; I < 7; ++I) {
            const int fld = (I < 6) ? 16 * I + c : (c < 4 ? 96 + c : 0);
            paddr[i][I] = (unsigned)ip[fld];
        }
    }

#pragma unroll
    for (int r = 1; r < 4; ++r) {
        char* dst = smem + LDS_SPARE + (((w + r) & 3) * 8192) + lane * 16;
#pragma unroll
        for (int h = 0; h < 2; ++h)
#pragma unroll
            for (int u = 0; u < 4; ++u)
                *reinterpret_cast<f32x4*>(dst + (h * 4 + u) * 1024) = acc[r * 2 + h][u];
        __syncthreads();
        const char* src = smem + LDS_SPARE + w * 8192 + lane * 16;
#pragma unroll
        for (int h = 0; h < 2; ++h)
#pragma unroll
            for (int u = 0; u < 4; ++u)
                acc[h][u] += *reinterpret_cast<const f32x4*>(src + (h * 4 + u) * 1024);
        if (r < 3) __syncthreads();
    }

    STAMP(4);
    const __amdgpu_buffer_rsrc_t xr = __builtin_amdgcn_make_buffer_rsrc(ws + WS_X, 0, 2048 * 4096, 0x00020000);
#pragma unroll
    for (int h = 0; h < 2; ++h) {
        const unsigned off = (unsigned)((((mt * 8 + h * 4 + w) * 8) + c8) * 4096 + lane * 16);
#pragma unroll
        for (int u = 0; u < 4; ++u)
            __builtin_amdgcn_raw_buffer_store_b128(__builtin_bit_cast(u32x4, acc[h][u]), xr, off + u * 1024, 0, 16);
    }

    STAMP(5);
#pragma unroll
    for (int t = 0; t < 28; ++t) asm volatile("" : "+v"(cf[t]));
#pragma unroll
    for (int i = 0; i < 4; ++i)
#pragma unroll
        for (int I = 0; I < 7; ++I) {
            unsigned v = paddr[i][I];
            if (I == 6) v = (c < 4) ? v : 1000u;
            paddr[i][I] = v * 128u + (((unsigned)q ^ ((v >> 1) & 7u)) << 4);
        }
    float lp[4];
    bf16x8 e[2][7][2];
#pragma unroll
    for (int I = 0; I < 7; ++I) {
        e[0][I][0] = lds_read128(smem, paddr[0][I]);
        e[0][I][1] = lds_read128(smem, paddr[0][I] ^ 64u);
    }
#pragma unroll
    for (int i = 0; i < 4; ++i) {
#pragma unroll
        for (int I = 0; I < 7; ++I) { asm volatile("" : "+a"(e[i & 1][I][0])); asm volatile("" : "+a"(e[i & 1][I][1])); }
        if (i < 3) {
#pragma unroll
            for (int I = 0; I < 7; ++I) {
                e[(i + 1) & 1][I][0] = lds_read128(smem, paddr[i + 1][I]);
                e[(i + 1) & 1][I][1] = lds_read128(smem, paddr[i + 1][I] ^ 64u);
            }
        }
        if (i == 3) {
            asm volatile("s_waitcnt vmcnt(0)" ::: "memory");
            if (lane == 0)
                __hip_atomic_store((GAS unsigned*)(ws + WS_FLAGS) + (bid * 4 + w), 1u, RLX_AGENT);
        }
        __builtin_amdgcn_sched_barrier(0);
        float s0 = 0.f, s1 = 0.f, s2 = 0.f, s3 = 0.f;
        f32x4 d[3];
        int t = 0;
#pragma unroll
        for (int I = 0; I < 7; ++I) {
#pragma unroll
            for (int J = I; J < 7; ++J, ++t) {
                if (t < 2) {
                    asm volatile(
                        "s_nop 1\n\t"
                        "v_mfma_f32_16x16x32_bf16 %0, %1, %2, 0\n\t"
                        "v_mfma_f32_16x16x32_bf16 %0, %3, %4, %0"
                        : "=&v"(d[t % 3])
                        : "a"(e[i & 1][I][0]), "a"(e[i & 1][J][0]), "a"(e[i & 1][I][1]), "a"(e[i & 1][J][1]));
                } else {
                    const f32x4 dp = d[(t + 1) % 3];
                    const f32x4 cp = cf[t - 2];
                    if (t == 2) {
                        asm volatile(
                            "v_mfma_f32_16x16x32_bf16 %0, %5, %6, 0\n\t"
                            "s_nop 4\n\t"
                            "v_fmac_f32 %1, %9, %13\n\t"
                            "v_fmac_f32 %2, %10, %14\n\t"
                            "v_mfma_f32_16x16x32_bf16 %0, %7, %8, %0\n\t"
                            "v_fmac_f32 %3, %11, %15\n\t"
                            "v_fmac_f32 %4, %12, %16"
                            : "=&v"(d[t % 3]), "+v"(s0), "+v"(s1), "+v"(s2), "+v"(s3)
                            : "a"(e[i & 1][I][0]), "a"(e[i & 1][J][0]), "a"(e[i & 1][I][1]), "a"(e[i & 1][J][1]),
                              "v"(cp[0]), "v"(cp[1]), "v"(cp[2]), "v"(cp[3]),
                              "v"(dp[0]), "v"(dp[1]), "v"(dp[2]), "v"(dp[3]));
                    } else {
                        asm volatile(
                            "v_mfma_f32_16x16x32_bf16 %0, %5, %6, 0\n\t"
                            "v_fmac_f32 %1, %9, %13\n\t"
                            "v_fmac_f32 %2, %10, %14\n\t"
                            "v_mfma_f32_16x16x32_bf16 %0, %7, %8, %0\n\t"
                            "v_fmac_f32 %3, %11, %15\n\t"
                            "v_fmac_f32 %4, %12, %16"
                            : "=&v"(d[t % 3]), "+v"(s0), "+v"(s1), "+v"(s2), "+v"(s3)
                            : "a"(e[i & 1][I][0]), "a"(e[i & 1][J][0]), "a"(e[i & 1][I][1]), "a"(e[i & 1][J][1]),
                              "v"(cp[0]), "v"(cp[1]), "v"(cp[2]), "v"(cp[3]),
                              "v"(dp[0]), "v"(dp[1]), "v"(dp[2]), "v"(dp[3]));
                    }
                }
            }
        }
        {
            const f32x4 da = d[26 % 3], db = d[27 % 3], ca = cf[26], cb = cf[27];
            asm volatile(
                "s_nop 1\n\t"
                "v_fmac_f32 %0, %4, %8\n\t"
                "v_fmac_f32 %1, %5, %9\n\t"
                "v_fmac_f32 %2, %6, %10\n\t"
                "v_fmac_f32 %3, %7, %11\n\t"
                "v_fmac_f32 %0, %12, %16\n\t"
                "v_fmac_f32 %1, %13, %17\n\t"
                "v_fmac_f32 %2, %14, %18\n\t"
                "v_fmac_f32 %3, %15, %19"
                : "+v"(s0), "+v"(s1), "+v"(s2), "+v"(s3)
                : "v"(ca[0]), "v"(ca[1]), "v"(ca[2]), "v"(ca[3]), "v"(da[0]), "v"(da[1]), "v"(da[2]), "v"(da[3]),
                  "v"(cb[0]), "v"(cb[1]), "v"(cb[2]), "v"(cb[3]), "v"(db[0]), "v"(db[1]), "v"(db[2]), "v"(db[3]));
        }
        lp[i] = (s0 + s1) + (s2 + s3);
        __builtin_amdgcn_sched_barrier(0);
    }
#pragma unroll
    for (int m = 1; m < 64; m <<= 1)
#pragma unroll
        for (int i = 0; i < 4; ++i) lp[i] += __shfl_xor(lp[i], m, 64);

    STAMP(6);
    f32x4 x[4];
    {
        GAS unsigned* fl = (GAS unsigned*)(ws + WS_FLAGS) + ((mt * 8 + w * 2) * 4 + (c8 & 3));
        unsigned spins = 0;
        for (;;) {
            bool ok = true;
            if (lane < 2) ok = __hip_atomic_load(fl + lane * 4, RLX_AGENT) == 1u;
            if (__all(ok)) break;
            __builtin_amdgcn_s_sleep(4);
            if (++spins > (1u << 22)) break;
        }
        __builtin_amdgcn_fence(__ATOMIC_ACQUIRE, "agent");
        const unsigned off = (unsigned)((((mt * 8 + c8) * 8) + w * 2) * 4096 + lane * 16);
#pragma unroll
        for (int u = 0; u < 4; ++u)
            x[u] = __builtin_bit_cast(f32x4, __builtin_amdgcn_raw_buffer_load_b128(xr, off + u * 1024, 0, 16)) +
                   __builtin_bit_cast(f32x4, __builtin_amdgcn_raw_buffer_load_b128(xr, off + 4096 + u * 1024, 0, 16));
    }

    STAMP(7);
    RSTAMP(11);
    {
        const int col0 = w * 64 + 4 * c;
        const u32x2 b1v = *reinterpret_cast<const u32x2*>(b1 + col0);
        const u32x2 w2v = *reinterpret_cast<const u32x2*>(w2 + col0);
        const float bb[4] = {__uint_as_float(b1v[0] << 16), __uint_as_float(b1v[0] & 0xffff0000u),
                             __uint_as_float(b1v[1] << 16), __uint_as_float(b1v[1] & 0xffff0000u)};
        const float ww[4] = {__uint_as_float(w2v[0] << 16), __uint_as_float(w2v[0] & 0xffff0000u),
                             __uint_as_float(w2v[1] << 16), __uint_as_float(w2v[1] & 0xffff0000u)};
        f32x4 rs;
#pragma unroll
        for (int r = 0; r < 4; ++r) {
            float a = 0.f;
#pragma unroll
            for (int u = 0; u < 4; ++u) {
                float y = bfround(x[u][r]);
                y = bfround(y + bb[u]);
                const float hh = bfround(gelu_tanh(y));
                a = fmaf(hh, ww[u], a);
            }
#pragma unroll
            for (int m = 1; m < 16; m <<= 1) a += __shfl_xor(a, m, 64);
            rs[r] = a + ((q == w) ? lp[r] : 0.f);
        }
        if (c == 0) *reinterpret_cast<f32x4*>(smem + LDS_RED + w * 64 + q * 16) = rs;
        __syncthreads();
        if (tid < 16) {
            const float* red = reinterpret_cast<const float*>(smem + LDS_RED);
            const float s = ((red[tid] + red[16 + tid]) + (red[32 + tid] + red[48 + tid])) + bf2f(b2[0]);
            out[frow0 + tid] = __float2bfloat16(s);
        }
    }
    STAMP(8);
    RSTAMP(12);
}

extern "C" void kernel_launch(void* const* d_in, const int* in_sizes, int n_in,
                              void* d_out, int out_size, void* d_ws, size_t ws_size,
                              hipStream_t stream) {
    const int* idx = (const int*)d_in[0];
    const unsigned short* table = (const unsigned short*)d_in[1];
    const unsigned short* wp = (const unsigned short*)d_in[2];
    const unsigned short* w1 = (const unsigned short*)d_in[3];
    const unsigned short* b1 = (const unsigned short*)d_in[4];
    const unsigned short* w2 = (const unsigned short*)d_in[5];
    const unsigned short* b2 = (const unsigned short*)d_in[6];
    dcp_prep<<<208, 256, 0, stream>>>(wp, w1, (char*)d_ws);
    dcp_kernel<<<256, 256, 0, stream>>>(idx, table, b1, w2, b2, (__hip_bfloat16*)d_out, (char*)d_ws);
}
```

```cpp
#include <hip/hip_runtime.h>
#include <hip/hip_bf16.h>
#include <stdint.h>

typedef __attribute__((ext_vector_type(8))) short bf16x8;
typedef __attribute__((ext_vector_type(4))) float f32x4;
typedef __attribute__((ext_vector_type(4))) unsigned u32x4;
typedef __attribute__((ext_vector_type(2))) unsigned u32x2;
typedef __attribute__((ext_vector_type(3), aligned(4))) int i32x3u;

#define GAS __attribute__((address_space(1)))
#define LAS __attribute__((address_space(3)))
#define RLX_AGENT __ATOMIC_RELAXED, __HIP_MEMORY_SCOPE_AGENT

constexpr int NF = 100;
constexpr int LDS_ZROW = 128000;
constexpr int LDS_SPARE = 128128;
constexpr int LDS_RED = LDS_SPARE + 32768;
constexpr int LDS_TOTAL = LDS_RED + 256;

constexpr size_t WS_FLAGS = 0;
constexpr size_t WS_COEF = 4096;
constexpr size_t WS_W1T = 65536;
constexpr size_t WS_X = WS_W1T + 200 * 16384;

#define RSTAMP(n) do {} while (0)
#define STAMP(n) do {} while (0)

__device__ __forceinline__ float bf2f(unsigned short u) { return __uint_as_float(((unsigned)u) << 16); }
__device__ __forceinline__ float bfround(float x) {
    return __bfloat162float(__float2bfloat16(x));
}
__device__ __forceinline__ float gelu_tanh(float x) {
    const float u = 0.7978845608028654f * (x + 0.044715f * x * x * x);
    const float e = __expf(2.0f * u);
    const float t = 1.0f - 2.0f * __builtin_amdgcn_rcpf(e + 1.0f);
    return 0.5f * x * (1.0f + t);
}
__device__ __forceinline__ bf16x8 lds_read128(const char* smem, unsigned off) {
    return *reinterpret_cast<const bf16x8*>(smem + off);
}

__global__ __launch_bounds__(256)
void dcp_prep(const unsigned short* __restrict__ wp, const unsigned short* __restrict__ w1,
              char* __restrict__ ws)
{
    const int lane = threadIdx.x & 63;
    const int gw = __builtin_amdgcn_readfirstlane(blockIdx.x * 4 + (threadIdx.x >> 6));
    const int c = lane & 15, q = lane >> 4;
    if (gw < 800) {
        const int nt = gw & 3, st = (gw >> 2) % 25, kw = (gw >> 2) / 25;
        const int U = kw * 4 + q;
        const int fld = st < 24 ? 3 * U + (st >> 3) : 96 + (U >> 3);
        const int chunk = st < 24 ? (st & 7) : (U & 7);
        const char* src = (const char*)w1 + ((size_t)fld * 64 + chunk * 8) * 512 + (nt * 64 + 4 * c) * 2;
        u32x2 r[8];
#pragma unroll
        for (int jj = 0; jj < 8; ++jj) r[jj] = *reinterpret_cast<const u32x2*>(src + jj * 512);
        u32x4 p0, p1, p2, p3;
#pragma unroll
        for (int p = 0; p < 4; ++p) {
            const u32x2 a = r[2 * p], b = r[2 * p + 1];
            p0[p] = __builtin_amdgcn_perm(b[0], a[0], 0x05040100u);
            p1[p] = __builtin_amdgcn_perm(b[0], a[0], 0x07060302u);
            p2[p] = __builtin_amdgcn_perm(b[1], a[1], 0x05040100u);
            p3[p] = __builtin_amdgcn_perm(b[1], a[1], 0x07060302u);
        }
        char* dst = ws + WS_W1T + (size_t)gw * 4096 + lane * 16;
        *reinterpret_cast<u32x4*>(dst) = p0;
        *reinterpret_cast<u32x4*>(dst + 1024) = p1;
        *reinterpret_cast<u32x4*>(dst + 2048) = p2;
        *reinterpret_cast<u32x4*>(dst + 3072) = p3;
    } else if (gw < 828) {
        const int t = gw - 800;
        const unsigned long long Ilo = 0x2221111110000000ull, Ihi = 0x0000655444333322ull;
        const unsigned long long Jlo = 0x4326543216543210ull, Jhi = 0x0000665654654365ull;
        const int I = (int)(((t < 16 ? Ilo : Ihi) >> ((t & 15) * 4)) & 15);
        const int J = (int)(((t < 16 ? Jlo : Jhi) >> ((t & 15) * 4)) & 15);
        const int jcol = 16 * J + c;
        f32x4 cfv;
#pragma unroll
        for (int r = 0; r < 4; ++r) {
            const int i = 16 * I + 4 * q + r;
            float v = 0.f;
            if (i < NF && jcol < NF) {
                v = bf2f(wp[i * NF + jcol]);
                if (I != J) v += bf2f(wp[jcol * NF + i]);
            }
            cfv[r] = v;
        }
        *reinterpret_cast<f32x4*>(ws + WS_COEF + t * 1024 + lane * 16) = cfv;
    } else if (gw == 828) {
#pragma unroll
        for (int k = 0; k < 4; ++k)
            *reinterpret_cast<u32x4*>(ws + WS_FLAGS + k * 1024 + lane * 16) = (u32x4){0u, 0u, 0u, 0u};
    }
}

__global__ __launch_bounds__(256, 1)
void dcp_kernel(const int* __restrict__ idx, const unsigned short* __restrict__ table,
                const unsigned short* __restrict__ b1, const unsigned short* __restrict__ w2,
                const unsigned short* __restrict__ b2, __hip_bfloat16* __restrict__ out,
                char* __restrict__ ws)
{
    __shared__ __attribute__((aligned(16))) char smem[LDS_TOTAL];

    const int tid = threadIdx.x;
    const int lane = tid & 63;
    const int w = __builtin_amdgcn_readfirstlane(tid >> 6);
    const int bid = blockIdx.x;
    const int mt = bid >> 3, c8 = bid & 7, nt = c8 >> 1, kh = bid & 1;
    const int c = lane & 15, q = lane >> 4;

    RSTAMP(10);
    STAMP(0);
    {
        const int srow = lane >> 3, slot = lane & 7;
        for (int p = w; p < 125; p += 4) {
            const int row = p * 8 + srow;
            const int chunk = slot ^ ((row >> 1) & 7);
            __builtin_amdgcn_global_load_lds(
                (const GAS unsigned*)((const char*)table + row * 128 + chunk * 16),
                (LAS unsigned*)(smem + p * 1024), 16, 0, 0);
        }
        if (tid < 8) *reinterpret_cast<u32x4*>(smem + LDS_ZROW + tid * 16) = (u32x4){0u, 0u, 0u, 0u};
    }

    const int kw = kh * 4 + w;
    const int U = kw * 4 + q;
    i32x3u iv[8]; int iv3[8];
#pragma unroll
    for (int j = 0; j < 4; ++j) {
#pragma unroll
        for (int h = 0; h < 2; ++h) {
            const int o = (w + j) & 3;
            const int sample = mt * 128 + h * 64 + o * 16 + c;
            const int* ip = idx + sample * NF;
            iv[j * 2 + h] = *reinterpret_cast<const i32x3u*>(ip + 3 * U);
            iv3[j * 2 + h] = ip[96 + (U >> 3)];
        }
    }
    const char* w1lane = ws + WS_W1T + ((size_t)kw * 100 + nt) * 4096 + lane * 16;
    bf16x8 bq[4][4];
#pragma unroll
    for (int s = 0; s < 4; ++s)
#pragma unroll
        for (int u = 0; u < 4; ++u)
            bq[s][u] = *reinterpret_cast<const bf16x8*>(w1lane + s * 16384 + u * 1024);
    u32x4 warm[4];
    {
        int id = mt * 4 + w; id = id < 100 ? id : 99;
        const char* wsrc = ws + WS_W1T + ((size_t)(kh * 100 + id) * 4 + nt) * 4096 + lane * 16;
#pragma unroll
        for (int u = 0; u < 4; ++u) warm[u] = *reinterpret_cast<const u32x4*>(wsrc + u * 1024);
    }
    __builtin_amdgcn_sched_barrier(0);

    unsigned addr[8][4];
#pragma unroll
    for (int t = 0; t < 8; ++t) {
#pragma unroll
        for (int fi = 0; fi < 4; ++fi) {
            const unsigned v = (unsigned)(fi < 3 ? iv[t][fi] : iv3[t]);
            unsigned a = v * 128u + (((v >> 1) & 7u) << 4);
            if (fi == 3) a ^= (unsigned)(U & 7) << 4;
            asm volatile("" : "+v"(a));
            addr[t][fi] = a;
        }
    }
#pragma unroll
    for (int u = 0; u < 4; ++u) asm volatile("" :: "v"(warm[u]));

    STAMP(1);
    __syncthreads();
    STAMP(2);

    f32x4 acc[8][4];
#pragma unroll
    for (int t = 0; t < 8; ++t)
#pragma unroll
        for (int u = 0; u < 4; ++u) acc[t][u] = (f32x4){0.f, 0.f, 0.f, 0.f};

    bf16x8 af[8];
#pragma unroll
    for (int t = 0; t < 8; ++t) af[t] = lds_read128(smem, addr[t][0]);
    __builtin_amdgcn_sched_barrier(0);

#pragma unroll
    for (int s = 0; s < 25; ++s) {
#pragma unroll
        for (int t = 0; t < 8; ++t) {
#pragma unroll
            for (int u = 0; u < 4; ++u)
                acc[t][u] = __builtin_amdgcn_mfma_f32_16x16x32_bf16(af[t], bq[s & 3][u], acc[t][u], 0, 0, 0);
            if (s + 1 < 24)
                af[t] = lds_read128(smem, addr[t][(s + 1) >> 3] ^ (unsigned)(((s + 1) & 7) << 4));
            else if (s + 1 == 24)
                af[t] = lds_read128(smem, addr[t][3]);
            if (s >= 1 && s + 3 < 25 && (t & 1) == 0) {
                bq[(s + 3) & 3][t >> 1] = *reinterpret_cast<const bf16x8*>(w1lane + (s + 3) * 16384 + (t >> 1) * 1024);
            }
            __builtin_amdgcn_sched_group_barrier(0x008, 1, 0);
            __builtin_amdgcn_sched_group_barrier(0x002, 1, 0);
            __builtin_amdgcn_sched_group_barrier(0x008, 1, 0);
            __builtin_amdgcn_sched_group_barrier(0x020, 1, 0);
            __builtin_amdgcn_sched_group_barrier(0x008, 1, 0);
            __builtin_amdgcn_sched_group_barrier(0x002, 1, 0);
            __builtin_amdgcn_sched_group_barrier(0x008, 1, 0);
            __builtin_amdgcn_sched_group_barrier(0x100, 1, 0);
        }
        __builtin_amdgcn_sched_barrier(0);
    }
    asm volatile("" ::: "memory");

    STAMP(3);
    f32x4 cf[28];
#pragma unroll
    for (int t = 0; t < 28; ++t)
        cf[t] = *reinterpret_cast<const f32x4*>(ws + WS_COEF + t * 1024 + lane * 16);

    const int frow0 = mt * 128 + c8 * 16;
    unsigned paddr[4][7];
#pragma unroll
    for (int i = 0; i < 4; ++i) {
        const int* ip = idx + (frow0 + w * 4 + i) * NF;
#pragma unroll
        for (int I = 0; I < 7; ++I) {
            const int fld = (I < 6) ? 16 * I + c : (c < 4 ? 96 + c : 0);
            paddr[i][I] = (unsigned)ip[fld];
        }
    }

#pragma unroll
    for (int r = 1; r < 4; ++r) {
        char* dst = smem + LDS_SPARE + (((w + r) & 3) * 8192) + lane * 16;
#pragma unroll
        for (int h = 0; h < 2; ++h)
#pragma unroll
            for (int u = 0; u < 4; ++u)
                *reinterpret_cast<f32x4*>(dst + (h * 4 + u) * 1024) = acc[r * 2 + h][u];
        __syncthreads();
        const char* src = smem + LDS_SPARE + w * 8192 + lane * 16;
#pragma unroll
        for (int h = 0; h < 2; ++h)
#pragma unroll
            for (int u = 0; u < 4; ++u)
                acc[h][u] += *reinterpret_cast<const f32x4*>(src + (h * 4 + u) * 1024);
        if (r < 3) __syncthreads();
    }

    STAMP(4);
    const __amdgpu_buffer_rsrc_t xr = __builtin_amdgcn_make_buffer_rsrc(ws + WS_X, 0, 2048 * 4096, 0x00020000);
#pragma unroll
    for (int h = 0; h < 2; ++h) {
        const unsigned off = (unsigned)((((mt * 8 + h * 4 + w) * 8) + c8) * 4096 + lane * 16);
#pragma unroll
        for (int u = 0; u < 4; ++u)
            __builtin_amdgcn_raw_buffer_store_b128(__builtin_bit_cast(u32x4, acc[h][u]), xr, off + u * 1024, 0, 16);
    }

    STAMP(5);
#pragma unroll
    for (int t = 0; t < 28; ++t) asm volatile("" : "+v"(cf[t]));
#pragma unroll
    for (int i = 0; i < 4; ++i)
#pragma unroll
        for (int I = 0; I < 7; ++I) {
            unsigned v = paddr[i][I];
            if (I == 6) v = (c < 4) ? v : 1000u;
            paddr[i][I] = v * 128u + (((unsigned)q ^ ((v >> 1) & 7u)) << 4);
        }
    float lp[4];
    bf16x8 e[2][7][2];
#pragma unroll
    for (int I = 0; I < 7; ++I) {
        e[0][I][0] = lds_read128(smem, paddr[0][I]);
        e[0][I][1] = lds_read128(smem, paddr[0][I] ^ 64u);
    }
#pragma unroll
    for (int i = 0; i < 4; ++i) {
#pragma unroll
        for (int I = 0; I < 7; ++I) { asm volatile("" : "+a"(e[i & 1][I][0])); asm volatile("" : "+a"(e[i & 1][I][1])); }
        if (i < 3) {
#pragma unroll
            for (int I = 0; I < 7; ++I) {
                e[(i + 1) & 1][I][0] = lds_read128(smem, paddr[i + 1][I]);
                e[(i + 1) & 1][I][1] = lds_read128(smem, paddr[i + 1][I] ^ 64u);
            }
        }
        if (i == 3) {
            asm volatile("s_waitcnt vmcnt(0)" ::: "memory");
            __syncthreads();
            if (tid == 0)
                __hip_atomic_store((GAS unsigned*)(ws + WS_FLAGS) + bid * 4, 1u, RLX_AGENT);
        }
        __builtin_amdgcn_sched_barrier(0);
        float s0 = 0.f, s1 = 0.f, s2 = 0.f, s3 = 0.f;
        f32x4 d[3];
        int t = 0;
#pragma unroll
        for (int I = 0; I < 7; ++I) {
#pragma unroll
            for (int J = I; J < 7; ++J, ++t) {
                if (t < 2) {
                    asm volatile(
                        "s_nop 1\n\t"
                        "v_mfma_f32_16x16x32_bf16 %0, %1, %2, 0\n\t"
                        "v_mfma_f32_16x16x32_bf16 %0, %3, %4, %0"
                        : "=&v"(d[t % 3])
                        : "a"(e[i & 1][I][0]), "a"(e[i & 1][J][0]), "a"(e[i & 1][I][1]), "a"(e[i & 1][J][1]));
                } else {
                    const f32x4 dp = d[(t + 1) % 3];
                    const f32x4 cp = cf[t - 2];
                    if (t == 2) {
                        asm volatile(
                            "v_mfma_f32_16x16x32_bf16 %0, %5, %6, 0\n\t"
                            "s_nop 4\n\t"
                            "v_fmac_f32 %1, %9, %13\n\t"
                            "v_fmac_f32 %2, %10, %14\n\t"
                            "v_mfma_f32_16x16x32_bf16 %0, %7, %8, %0\n\t"
                            "v_fmac_f32 %3, %11, %15\n\t"
                            "v_fmac_f32 %4, %12, %16"
                            : "=&v"(d[t % 3]), "+v"(s0), "+v"(s1), "+v"(s2), "+v"(s3)
                            : "a"(e[i & 1][I][0]), "a"(e[i & 1][J][0]), "a"(e[i & 1][I][1]), "a"(e[i & 1][J][1]),
                              "v"(cp[0]), "v"(cp[1]), "v"(cp[2]), "v"(cp[3]),
                              "v"(dp[0]), "v"(dp[1]), "v"(dp[2]), "v"(dp[3]));
                    } else {
                        asm volatile(
                            "v_mfma_f32_16x16x32_bf16 %0, %5, %6, 0\n\t"
                            "v_fmac_f32 %1, %9, %13\n\t"
                            "v_fmac_f32 %2, %10, %14\n\t"
                            "v_mfma_f32_16x16x32_bf16 %0, %7, %8, %0\n\t"
                            "v_fmac_f32 %3, %11, %15\n\t"
                            "v_fmac_f32 %4, %12, %16"
                            : "=&v"(d[t % 3]), "+v"(s0), "+v"(s1), "+v"(s2), "+v"(s3)
                            : "a"(e[i & 1][I][0]), "a"(e[i & 1][J][0]), "a"(e[i & 1][I][1]), "a"(e[i & 1][J][1]),
                              "v"(cp[0]), "v"(cp[1]), "v"(cp[2]), "v"(cp[3]),
                              "v"(dp[0]), "v"(dp[1]), "v"(dp[2]), "v"(dp[3]));
                    }
                }
            }
        }
        {
            const f32x4 da = d[26 % 3], db = d[27 % 3], ca = cf[26], cb = cf[27];
            asm volatile(
                "s_nop 1\n\t"
                "v_fmac_f32 %0, %4, %8\n\t"
                "v_fmac_f32 %1, %5, %9\n\t"
                "v_fmac_f32 %2, %6, %10\n\t"
                "v_fmac_f32 %3, %7, %11\n\t"
                "v_fmac_f32 %0, %12, %16\n\t"
                "v_fmac_f32 %1, %13, %17\n\t"
                "v_fmac_f32 %2, %14, %18\n\t"
                "v_fmac_f32 %3, %15, %19"
                : "+v"(s0), "+v"(s1), "+v"(s2), "+v"(s3)
                : "v"(ca[0]), "v"(ca[1]), "v"(ca[2]), "v"(ca[3]), "v"(da[0]), "v"(da[1]), "v"(da[2]), "v"(da[3]),
                  "v"(cb[0]), "v"(cb[1]), "v"(cb[2]), "v"(cb[3]), "v"(db[0]), "v"(db[1]), "v"(db[2]), "v"(db[3]));
        }
        lp[i] = (s0 + s1) + (s2 + s3);
        __builtin_amdgcn_sched_barrier(0);
    }
#pragma unroll
    for (int m = 1; m < 64; m <<= 1)
#pragma unroll
        for (int i = 0; i < 4; ++i) lp[i] += __shfl_xor(lp[i], m, 64);

    STAMP(6);
    f32x4 x[4];
    {
        GAS unsigned* fl = (GAS unsigned*)(ws + WS_FLAGS) + (mt * 8 + w * 2) * 4;
        unsigned spins = 0;
        for (;;) {
            bool ok = true;
            if (lane < 2) ok = __hip_atomic_load(fl + lane * 4, RLX_AGENT) == 1u;
            if (__all(ok)) break;
            __builtin_amdgcn_s_sleep(4);
            if (++spins > (1u << 22)) break;
        }
        __builtin_amdgcn_fence(__ATOMIC_ACQUIRE, "wavefront");
        const unsigned off = (unsigned)((((mt * 8 + c8) * 8) + w * 2) * 4096 + lane * 16);
#pragma unroll
        for (int u = 0; u < 4; ++u)
            x[u] = __builtin_bit_cast(f32x4, __builtin_amdgcn_raw_buffer_load_b128(xr, off + u * 1024, 0, 16)) +
                   __builtin_bit_cast(f32x4, __builtin_amdgcn_raw_buffer_load_b128(xr, off + 4096 + u * 1024, 0, 16));
    }

    STAMP(7);
    RSTAMP(11);
    {
        const int col0 = w * 64 + 4 * c;
        const u32x2 b1v = *reinterpret_cast<const u32x2*>(b1 + col0);
        const u32x2 w2v = *reinterpret_cast<const u32x2*>(w2 + col0);
        const float bb[4] = {__uint_as_float(b1v[0] << 16), __uint_as_float(b1v[0] & 0xffff0000u),
                             __uint_as_float(b1v[1] << 16), __uint_as_float(b1v[1] & 0xffff0000u)};
        const float ww[4] = {__uint_as_float(w2v[0] << 16), __uint_as_float(w2v[0] & 0xffff0000u),
                             __uint_as_float(w2v[1] << 16), __uint_as_float(w2v[1] & 0xffff0000u)};
        f32x4 rs;
#pragma unroll
        for (int r = 0; r < 4; ++r) {
            float a = 0.f;
#pragma unroll
            for (int u = 0; u < 4; ++u) {
                float y = bfround(x[u][r]);
                y = bfround(y + bb[u]);
                const float hh = bfround(gelu_tanh(y));
                a = fmaf(hh, ww[u], a);
            }
#pragma unroll
            for (int m = 1; m < 16; m <<= 1) a += __shfl_xor(a, m, 64);
            rs[r] = a + ((q == w) ? lp[r] : 0.f);
        }
        if (c == 0) *reinterpret_cast<f32x4*>(smem + LDS_RED + w * 64 + q * 16) = rs;
        __syncthreads();
        if (tid < 16) {
            const float* red = reinterpret_cast<const float*>(smem + LDS_RED);
            const float s = ((red[tid] + red[16 + tid]) + (red[32 + tid] + red[48 + tid])) + bf2f(b2[0]);
            out[frow0 + tid] = __float2bfloat16(s);
        }
    }
    STAMP(8);
    RSTAMP(12);
}

extern "C" void kernel_launch(void* const* d_in, const int* in_sizes, int n_in,
                              void* d_out, int out_size, void* d_ws, size_t ws_size,
                              hipStream_t stream) {
    const int* idx = (const int*)d_in[0];
    const unsigned short* table = (const unsigned short*)d_in[1];
    const unsigned short* wp = (const unsigned short*)d_in[2];
    const unsigned short* w1 = (const unsigned short*)d_in[3];
    const unsigned short* b1 = (const unsigned short*)d_in[4];
    const unsigned short* w2 = (const unsigned short*)d_in[5];
    const unsigned short* b2 = (const unsigned short*)d_in[6];
    dcp_prep<<<208, 256, 0, stream>>>(wp, w1, (char*)d_ws);
    dcp_kernel<<<256, 256, 0, stream>>>(idx, table, b1, w2, b2, (__hip_bfloat16*)d_out, (char*)d_ws);
}
```

```cpp
#include <hip/hip_runtime.h>
#include <hip/hip_bf16.h>
#include <stdint.h>

typedef __attribute__((ext_vector_type(8))) short bf16x8;
typedef __attribute__((ext_vector_type(4))) float f32x4;
typedef __attribute__((ext_vector_type(4))) unsigned u32x4;
typedef __attribute__((ext_vector_type(2))) unsigned u32x2;
typedef __attribute__((ext_vector_type(3), aligned(4))) int i32x3u;

#define GAS __attribute__((address_space(1)))
#define LAS __attribute__((address_space(3)))
#define RLX_AGENT __ATOMIC_RELAXED, __HIP_MEMORY_SCOPE_AGENT

constexpr int NF = 100;
constexpr int LDS_ZROW = 128000;
constexpr int LDS_SPARE = 128128;
constexpr int LDS_TOTAL = LDS_SPARE + 32768;

constexpr size_t WS_FLAGS = 0;
constexpr size_t WS_COEF = 4096;
constexpr size_t WS_GRAN = 65536;
constexpr size_t WS_W1T = 262144;
constexpr size_t WS_X = WS_W1T + 200 * 16384;

#define RSTAMP(n) do {} while (0)
#define STAMP(n) do {} while (0)

__device__ __forceinline__ float bf2f(unsigned short u) { return __uint_as_float(((unsigned)u) << 16); }
__device__ __forceinline__ float bfround(float x) {
    return __bfloat162float(__float2bfloat16(x));
}
__device__ __forceinline__ float gelu_tanh(float x) {
    const float u = 0.7978845608028654f * (x + 0.044715f * x * x * x);
    const float e = __expf(2.0f * u);
    const float t = 1.0f - 2.0f * __builtin_amdgcn_rcpf(e + 1.0f);
    return 0.5f * x * (1.0f + t);
}
__device__ __forceinline__ bf16x8 lds_read128(const char* smem, unsigned off) {
    return *reinterpret_cast<const bf16x8*>(smem + off);
}

__global__ __launch_bounds__(256)
void dcp_prep(const unsigned short* __restrict__ wp, const unsigned short* __restrict__ w1,
              char* __restrict__ ws)
{
    const int lane = threadIdx.x & 63;
    const int gw = __builtin_amdgcn_readfirstlane(blockIdx.x * 4 + (threadIdx.x >> 6));
    const int c = lane & 15, q = lane >> 4;
    if (gw < 800) {
        const int nt = gw & 3, st = (gw >> 2) % 25, kw = (gw >> 2) / 25;
        const int U = kw * 4 + q;
        const int fld = st < 24 ? 3 * U + (st >> 3) : 96 + (U >> 3);
        const int chunk = st < 24 ? (st & 7) : (U & 7);
        const char* src = (const char*)w1 + ((size_t)fld * 64 + chunk * 8) * 512 + (nt * 64 + 4 * c) * 2;
        u32x2 r[8];
#pragma unroll
        for (int jj = 0; jj < 8; ++jj) r[jj] = *reinterpret_cast<const u32x2*>(src + jj * 512);
        u32x4 p0, p1, p2, p3;
#pragma unroll
        for (int p = 0; p < 4; ++p) {
            const u32x2 a = r[2 * p], b = r[2 * p + 1];
            p0[p] = __builtin_amdgcn_perm(b[0], a[0], 0x05040100u);
            p1[p] = __builtin_amdgcn_perm(b[0], a[0], 0x07060302u);
            p2[p] = __builtin_amdgcn_perm(b[1], a[1], 0x05040100u);
            p3[p] = __builtin_amdgcn_perm(b[1], a[1], 0x07060302u);
        }
        char* dst = ws + WS_W1T + (size_t)gw * 4096 + lane * 16;
        *reinterpret_cast<u32x4*>(dst) = p0;
        *reinterpret_cast<u32x4*>(dst + 1024) = p1;
        *reinterpret_cast<u32x4*>(dst + 2048) = p2;
        *reinterpret_cast<u32x4*>(dst + 3072) = p3;
    } else if (gw < 828) {
        const int t = gw - 800;
        const unsigned long long Ilo = 0x2221111110000000ull, Ihi = 0x0000655444333322ull;
        const unsigned long long Jlo = 0x4326543216543210ull, Jhi = 0x0000665654654365ull;
        const int I = (int)(((t < 16 ? Ilo : Ihi) >> ((t & 15) * 4)) & 15);
        const int J = (int)(((t < 16 ? Jlo : Jhi) >> ((t & 15) * 4)) & 15);
        const int jcol = 16 * J + c;
        f32x4 cfv;
#pragma unroll
        for (int r = 0; r < 4; ++r) {
            const int i = 16 * I + 4 * q + r;
            float v = 0.f;
            if (i < NF && jcol < NF) {
                v = bf2f(wp[i * NF + jcol]);
                if (I != J) v += bf2f(wp[jcol * NF + i]);
            }
            cfv[r] = v;
        }
        *reinterpret_cast<f32x4*>(ws + WS_COEF + t * 1024 + lane * 16) = cfv;
    } else if (gw == 828) {
#pragma unroll
        for (int k = 0; k < 4; ++k)
            *reinterpret_cast<u32x4*>(ws + WS_FLAGS + k * 1024 + lane * 16) = (u32x4){0u, 0u, 0u, 0u};
    } else if (gw < 861) {
#pragma unroll
        for (int k = 0; k < 4; ++k)
            *reinterpret_cast<u32x4*>(ws + WS_GRAN + (gw - 829) * 4096 + k * 1024 + lane * 16) = (u32x4){0u, 0u, 0u, 0u};
    }
}

__global__ __launch_bounds__(256, 1)
void dcp_kernel(const int* __restrict__ idx, const unsigned short* __restrict__ table,
                const unsigned short* __restrict__ b1, const unsigned short* __restrict__ w2,
                const unsigned short* __restrict__ b2, __hip_bfloat16* __restrict__ out,
                char* __restrict__ ws)
{
    __shared__ __attribute__((aligned(16))) char smem[LDS_TOTAL];

    const int tid = threadIdx.x;
    const int lane = tid & 63;
    const int w = __builtin_amdgcn_readfirstlane(tid >> 6);
    const int bid = blockIdx.x;
    const int mt = bid >> 3, c8 = bid & 7, nt = c8 >> 1, kh = bid & 1;
    const int c = lane & 15, q = lane >> 4;

    RSTAMP(10);
    STAMP(0);
    {
        const int srow = lane >> 3, slot = lane & 7;
        for (int p = w; p < 125; p += 4) {
            const int row = p * 8 + srow;
            const int chunk = slot ^ ((row >> 1) & 7);
            __builtin_amdgcn_global_load_lds(
                (const GAS unsigned*)((const char*)table + row * 128 + chunk * 16),
                (LAS unsigned*)(smem + p * 1024), 16, 0, 0);
        }
        if (tid < 8) *reinterpret_cast<u32x4*>(smem + LDS_ZROW + tid * 16) = (u32x4){0u, 0u, 0u, 0u};
    }

    const int kw = kh * 4 + w;
    const int U = kw * 4 + q;
    i32x3u iv[8]; int iv3[8];
#pragma unroll
    for (int j = 0; j < 4; ++j) {
#pragma unroll
        for (int h = 0; h < 2; ++h) {
            const int o = (w + j) & 3;
            const int sample = mt * 128 + (h ^ kh) * 64 + o * 16 + c;
            const int* ip = idx + sample * NF;
            iv[j * 2 + h] = *reinterpret_cast<const i32x3u*>(ip + 3 * U);
            iv3[j * 2 + h] = ip[96 + (U >> 3)];
        }
    }
    const char* w1lane = ws + WS_W1T + ((size_t)kw * 100 + nt) * 4096 + lane * 16;
    bf16x8 bq[4][4];
#pragma unroll
    for (int s = 0; s < 4; ++s)
#pragma unroll
        for (int u = 0; u < 4; ++u)
            bq[s][u] = *reinterpret_cast<const bf16x8*>(w1lane + s * 16384 + u * 1024);
    u32x4 warm[4];
    {
        int id = mt * 4 + w; id = id < 100 ? id : 99;
        const char* wsrc = ws + WS_W1T + ((size_t)(kh * 100 + id) * 4 + nt) * 4096 + lane * 16;
#pragma unroll
        for (int u = 0; u < 4; ++u) warm[u] = *reinterpret_cast<const u32x4*>(wsrc + u * 1024);
    }
    __builtin_amdgcn_sched_barrier(0);

    unsigned addr[8][4];
#pragma unroll
    for (int t = 0; t < 8; ++t) {
#pragma unroll
        for (int fi = 0; fi < 4; ++fi) {
            const unsigned v = (unsigned)(fi < 3 ? iv[t][fi] : iv3[t]);
            unsigned a = v * 128u + (((v >> 1) & 7u) << 4);
            if (fi == 3) a ^= (unsigned)(U & 7) << 4;
            asm volatile("" : "+v"(a));
            addr[t][fi] = a;
        }
    }
#pragma unroll
    for (int u = 0; u < 4; ++u) asm volatile("" :: "v"(warm[u]));

    STAMP(1);
    __syncthreads();
    STAMP(2);

    f32x4 acc[8][4];
#pragma unroll
    for (int t = 0; t < 8; ++t)
#pragma unroll
        for (int u = 0; u < 4; ++u) acc[t][u] = (f32x4){0.f, 0.f, 0.f, 0.f};

    bf16x8 af[8];
#pragma unroll
    for (int t = 0; t < 8; ++t) af[t] = lds_read128(smem, addr[t][0]);
    const int prow0 = mt * 128 + kh * 64 + w * 16;
    unsigned paddr[4][7];
    __builtin_amdgcn_sched_barrier(0);

#pragma unroll
    for (int s = 0; s < 25; ++s) {
#pragma unroll
        for (int t = 0; t < 8; ++t) {
#pragma unroll
            for (int u = 0; u < 4; ++u)
                acc[t][u] = __builtin_amdgcn_mfma_f32_16x16x32_bf16(af[t], bq[s & 3][u], acc[t][u], 0, 0, 0);
            if (s + 1 < 24)
                af[t] = lds_read128(smem, addr[t][(s + 1) >> 3] ^ (unsigned)(((s + 1) & 7) << 4));
            else if (s + 1 == 24)
                af[t] = lds_read128(smem, addr[t][3]);
            if (s >= 1 && s + 3 < 25 && (t & 1) == 0) {
                bq[(s + 3) & 3][t >> 1] = *reinterpret_cast<const bf16x8*>(w1lane + (s + 3) * 16384 + (t >> 1) * 1024);
            }
            if (s >= 2 && s < 6 && t < 7) {
                const int fld = (t < 6) ? 16 * t + c : (c < 4 ? 96 + c : 0);
                paddr[s - 2][t] = (unsigned)idx[(prow0 + nt * 4 + (s - 2)) * NF + fld];
            }
            if (s >= 12 && s < 16 && t < 7) {
                unsigned v = paddr[s - 12][t];
                if (t == 6) v = (c < 4) ? v : 1000u;
                unsigned a = v * 128u + (((unsigned)q ^ ((v >> 1) & 7u)) << 4);
                asm volatile("" : "+v"(a));
                paddr[s - 12][t] = a;
            }
            __builtin_amdgcn_sched_group_barrier(0x008, 1, 0);
            __builtin_amdgcn_sched_group_barrier(0x002, 2, 0);
            __builtin_amdgcn_sched_group_barrier(0x008, 1, 0);
            __builtin_amdgcn_sched_group_barrier(0x020, 1, 0);
            __builtin_amdgcn_sched_group_barrier(0x002, 1, 0);
            __builtin_amdgcn_sched_group_barrier(0x008, 1, 0);
            __builtin_amdgcn_sched_group_barrier(0x002, 2, 0);
            __builtin_amdgcn_sched_group_barrier(0x008, 1, 0);
            __builtin_amdgcn_sched_group_barrier(0x100, 1, 0);
            __builtin_amdgcn_sched_group_barrier(0x020, 1, 0);
        }
        __builtin_amdgcn_sched_barrier(0);
    }
    asm volatile("" ::: "memory");

    STAMP(3);
    f32x4 cf[28];
#pragma unroll
    for (int r = 1; r < 4; ++r) {
        char* dst = smem + LDS_SPARE + (((w + r) & 3) * 8192) + lane * 16;
#pragma unroll
        for (int h = 0; h < 2; ++h)
#pragma unroll
            for (int u = 0; u < 4; ++u)
                *reinterpret_cast<f32x4*>(dst + (h * 4 + u) * 1024) = acc[r * 2 + h][u];
        if (r == 1) {
            __builtin_amdgcn_sched_barrier(0);
#pragma unroll
            for (int t = 0; t < 28; ++t)
                cf[t] = *reinterpret_cast<const f32x4*>(ws + WS_COEF + t * 1024 + lane * 16);
            __builtin_amdgcn_sched_barrier(0);
        }
        __syncthreads();
        const char* src = smem + LDS_SPARE + w * 8192 + lane * 16;
#pragma unroll
        for (int h = 0; h < 2; ++h)
#pragma unroll
            for (int u = 0; u < 4; ++u)
                acc[h][u] += *reinterpret_cast<const f32x4*>(src + (h * 4 + u) * 1024);
        if (r < 3) __syncthreads();
    }

    STAMP(4);
    const __amdgpu_buffer_rsrc_t xr = __builtin_amdgcn_make_buffer_rsrc(ws + WS_X, 0, 1024 * 4096, 0x00020000);
    {
        const unsigned off = (unsigned)((bid * 4 + w) * 4096 + lane * 16);
#pragma unroll
        for (int u = 0; u < 4; ++u)
            __builtin_amdgcn_raw_buffer_store_b128(__builtin_bit_cast(u32x4, acc[1][u]), xr, off + u * 1024, 0, 16);
    }

    STAMP(5);
#pragma unroll
    for (int t = 0; t < 28; ++t) asm volatile("" : "+v"(cf[t]));
    float lp[4];
    GAS unsigned long long* gr = (GAS unsigned long long*)(ws + WS_GRAN) + ((mt * 2 + kh) * 4 + w) * 64;
    bf16x8 e[2][7][2];
#pragma unroll
    for (int I = 0; I < 7; ++I) {
        e[0][I][0] = lds_read128(smem, paddr[0][I]);
        e[0][I][1] = lds_read128(smem, paddr[0][I] ^ 64u);
    }
#pragma unroll
    for (int i = 0; i < 4; ++i) {
#pragma unroll
        for (int I = 0; I < 7; ++I) { asm volatile("" : "+a"(e[i & 1][I][0])); asm volatile("" : "+a"(e[i & 1][I][1])); }
        if (i < 3) {
#pragma unroll
            for (int I = 0; I < 7; ++I) {
                e[(i + 1) & 1][I][0] = lds_read128(smem, paddr[i + 1][I]);
                e[(i + 1) & 1][I][1] = lds_read128(smem, paddr[i + 1][I] ^ 64u);
            }
        }
        if (i == 2) {
            asm volatile("s_waitcnt vmcnt(0)" ::: "memory");
            __syncthreads();
            if (tid == 0)
                __hip_atomic_store((GAS unsigned*)(ws + WS_FLAGS) + bid * 4, 1u, RLX_AGENT);
        }
        if (i == 3) {
            GAS unsigned* fl = (GAS unsigned*)(ws + WS_FLAGS) + (bid ^ 1) * 4;
            unsigned spins = 0;
            while ((unsigned)__builtin_amdgcn_readfirstlane(__hip_atomic_load(fl, RLX_AGENT)) != 1u) {
                __builtin_amdgcn_s_sleep(4);
                if (++spins > (1u << 22)) break;
            }
            __builtin_amdgcn_fence(__ATOMIC_ACQUIRE, "wavefront");
            const unsigned off = (unsigned)(((bid ^ 1) * 4 + w) * 4096 + lane * 16);
#pragma unroll
            for (int u = 0; u < 4; ++u)
                acc[0][u] += __builtin_bit_cast(f32x4, __builtin_amdgcn_raw_buffer_load_b128(xr, off + u * 1024, 0, 16));
            const int col0 = nt * 64 + 4 * c;
            const u32x2 b1v = *reinterpret_cast<const u32x2*>(b1 + col0);
            const u32x2 w2v = *reinterpret_cast<const u32x2*>(w2 + col0);
            const float bb[4] = {__uint_as_float(b1v[0] << 16), __uint_as_float(b1v[0] & 0xffff0000u),
                                 __uint_as_float(b1v[1] << 16), __uint_as_float(b1v[1] & 0xffff0000u)};
            const float ww[4] = {__uint_as_float(w2v[0] << 16), __uint_as_float(w2v[0] & 0xffff0000u),
                                 __uint_as_float(w2v[1] << 16), __uint_as_float(w2v[1] & 0xffff0000u)};
            float rs[4];
#pragma unroll
            for (int r = 0; r < 4; ++r) {
                float a = 0.f;
#pragma unroll
                for (int u = 0; u < 4; ++u) {
                    float y = bfround(acc[0][u][r]);
                    y = bfround(y + bb[u]);
                    const float hh = bfround(gelu_tanh(y));
                    a = fmaf(hh, ww[u], a);
                }
                rs[r] = a;
            }
#pragma unroll
            for (int m = 1; m < 16; m <<= 1)
#pragma unroll
                for (int r = 0; r < 4; ++r) rs[r] += __shfl_xor(rs[r], m, 64);
            if (c == 0) {
#pragma unroll
                for (int r = 0; r < 4; ++r)
                    __hip_atomic_store(gr + nt * 16 + q * 4 + r, (1ull << 32) | (unsigned long long)__float_as_uint(rs[r]), RLX_AGENT);
            }
        }
        __builtin_amdgcn_sched_barrier(0);
        if (i == 0) RSTAMP(9);
        if (i == 1) RSTAMP(13);
        if (i == 2) RSTAMP(14);
        if (i == 3) RSTAMP(15);
        float s0 = 0.f, s1 = 0.f, s2 = 0.f, s3 = 0.f;
        f32x4 d[3];
        int t = 0;
#pragma unroll
        for (int I = 0; I < 7; ++I) {
#pragma unroll
            for (int J = I; J < 7; ++J, ++t) {
                if (t < 2) {
                    asm volatile(
                        "s_nop 1\n\t"
                        "v_mfma_f32_16x16x32_bf16 %0, %1, %2, 0\n\t"
                        "v_mfma_f32_16x16x32_bf16 %0, %3, %4, %0"
                        : "=&v"(d[t % 3])
                        : "a"(e[i & 1][I][0]), "a"(e[i & 1][J][0]), "a"(e[i & 1][I][1]), "a"(e[i & 1][J][1]));
                } else {
                    const f32x4 dp = d[(t + 1) % 3];
                    const f32x4 cp = cf[t - 2];
                    if (t == 2) {
                        asm volatile(
                            "v_mfma_f32_16x16x32_bf16 %0, %5, %6, 0\n\t"
                            "s_nop 4\n\t"
                            "v_fmac_f32 %1, %9, %13\n\t"
                            "v_fmac_f32 %2, %10, %14\n\t"
                            "v_mfma_f32_16x16x32_bf16 %0, %7, %8, %0\n\t"
                            "v_fmac_f32 %3, %11, %15\n\t"
                            "v_fmac_f32 %4, %12, %16"
                            : "=&v"(d[t % 3]), "+v"(s0), "+v"(s1), "+v"(s2), "+v"(s3)
                            : "a"(e[i & 1][I][0]), "a"(e[i & 1][J][0]), "a"(e[i & 1][I][1]), "a"(e[i & 1][J][1]),
                              "v"(cp[0]), "v"(cp[1]), "v"(cp[2]), "v"(cp[3]),
                              "v"(dp[0]), "v"(dp[1]), "v"(dp[2]), "v"(dp[3]));
                    } else {
                        asm volatile(
                            "v_mfma_f32_16x16x32_bf16 %0, %5, %6, 0\n\t"
                            "v_fmac_f32 %1, %9, %13\n\t"
                            "v_fmac_f32 %2, %10, %14\n\t"
                            "v_mfma_f32_16x16x32_bf16 %0, %7, %8, %0\n\t"
                            "v_fmac_f32 %3, %11, %15\n\t"
                            "v_fmac_f32 %4, %12, %16"
                            : "=&v"(d[t % 3]), "+v"(s0), "+v"(s1), "+v"(s2), "+v"(s3)
                            : "a"(e[i & 1][I][0]), "a"(e[i & 1][J][0]), "a"(e[i & 1][I][1]), "a"(e[i & 1][J][1]),
                              "v"(cp[0]), "v"(cp[1]), "v"(cp[2]), "v"(cp[3]),
                              "v"(dp[0]), "v"(dp[1]), "v"(dp[2]), "v"(dp[3]));
                    }
                }
            }
        }
        {
            const f32x4 da = d[26 % 3], db = d[27 % 3], ca = cf[26], cb = cf[27];
            asm volatile(
                "s_nop 1\n\t"
                "v_fmac_f32 %0, %4, %8\n\t"
                "v_fmac_f32 %1, %5, %9\n\t"
                "v_fmac_f32 %2, %6, %10\n\t"
                "v_fmac_f32 %3, %7, %11\n\t"
                "v_fmac_f32 %0, %12, %16\n\t"
                "v_fmac_f32 %1, %13, %17\n\t"
                "v_fmac_f32 %2, %14, %18\n\t"
                "v_fmac_f32 %3, %15, %19"
                : "+v"(s0), "+v"(s1), "+v"(s2), "+v"(s3)
                : "v"(ca[0]), "v"(ca[1]), "v"(ca[2]), "v"(ca[3]), "v"(da[0]), "v"(da[1]), "v"(da[2]), "v"(da[3]),
                  "v"(cb[0]), "v"(cb[1]), "v"(cb[2]), "v"(cb[3]), "v"(db[0]), "v"(db[1]), "v"(db[2]), "v"(db[3]));
        }
        lp[i] = (s0 + s1) + (s2 + s3);
        __builtin_amdgcn_sched_barrier(0);
    }
#pragma unroll
    for (int m = 1; m < 64; m <<= 1)
#pragma unroll
        for (int i = 0; i < 4; ++i) lp[i] += __shfl_xor(lp[i], m, 64);

    STAMP(6);
    {
        float v = 0.f;
        unsigned spins = 0;
        for (;;) {
            bool ok = true;
            if (lane < 16) {
                const unsigned long long xg = __hip_atomic_load(gr + (lane >> 2) * 16 + nt * 4 + (lane & 3), RLX_AGENT);
                v = __uint_as_float((unsigned)xg);
                ok = (xg >> 32) == 1ull;
            }
            if (__all(ok)) break;
            __builtin_amdgcn_s_sleep(8);
            if (++spins > (1u << 22)) break;
        }
        STAMP(7);
        RSTAMP(11);
        v += __shfl_xor(v, 4, 64);
        v += __shfl_xor(v, 8, 64);
        const float lpv = (lane & 2) ? ((lane & 1) ? lp[3] : lp[2]) : ((lane & 1) ? lp[1] : lp[0]);
        if (lane < 4) out[prow0 + nt * 4 + lane] = __float2bfloat16(v + bf2f(b2[0]) + lpv);
    }
    STAMP(8);
    RSTAMP(12);
}

extern "C" void kernel_launch(void* const* d_in, const int* in_sizes, int n_in,
                              void* d_out, int out_size, void* d_ws, size_t ws_size,
                              hipStream_t stream) {
    const int* idx = (const int*)d_in[0];
    const unsigned short* table = (const unsigned short*)d_in[1];
    const unsigned short* wp = (const unsigned short*)d_in[2];
    const unsigned short* w1 = (const unsigned short*)d_in[3];
    const unsigned short* b1 = (const unsigned short*)d_in[4];
    const unsigned short* w2 = (const unsigned short*)d_in[5];
    const unsigned short* b2 = (const unsigned short*)d_in[6];
    dcp_prep<<<216, 256, 0, stream>>>(wp, w1, (char*)d_ws);
    dcp_kernel<<<256, 256, 0, stream>>>(idx, table, b1, w2, b2, (__hip_bfloat16*)d_out, (char*)d_ws);
}
```

```cpp
#include <hip/hip_runtime.h>
#include <hip/hip_bf16.h>
#include <stdint.h>

typedef __attribute__((ext_vector_type(8))) short bf16x8;
typedef __attribute__((ext_vector_type(4))) float f32x4;
typedef __attribute__((ext_vector_type(4))) unsigned u32x4;
typedef __attribute__((ext_vector_type(2))) unsigned u32x2;
typedef __attribute__((ext_vector_type(3), aligned(4))) int i32x3u;

#define GAS __attribute__((address_space(1)))
#define LAS __attribute__((address_space(3)))
#define RLX_AGENT __ATOMIC_RELAXED, __HIP_MEMORY_SCOPE_AGENT

constexpr int NF = 100;
constexpr int LDS_ZROW = 128000;
constexpr int LDS_SPARE = 128128;
constexpr int LDS_TOTAL = LDS_SPARE + 32768;

constexpr size_t WS_FLAGS = 0;
constexpr size_t WS_COEF = 4096;
constexpr size_t WS_GRAN = 65536;
constexpr size_t WS_W1T = 262144;
constexpr size_t WS_X = WS_W1T + 200 * 16384;

#define RSTAMP(n) do {} while (0)
#define STAMP(n) do {} while (0)

__device__ __forceinline__ float bf2f(unsigned short u) { return __uint_as_float(((unsigned)u) << 16); }
__device__ __forceinline__ float bfround(float x) {
    return __bfloat162float(__float2bfloat16(x));
}
__device__ __forceinline__ float gelu_tanh(float x) {
    const float u = 0.7978845608028654f * (x + 0.044715f * x * x * x);
    const float e = __expf(2.0f * u);
    const float t = 1.0f - 2.0f * __builtin_amdgcn_rcpf(e + 1.0f);
    return 0.5f * x * (1.0f + t);
}
__device__ __forceinline__ bf16x8 lds_read128(const char* smem, unsigned off) {
    return *reinterpret_cast<const bf16x8*>(smem + off);
}

__global__ __launch_bounds__(256)
void dcp_prep(const unsigned short* __restrict__ wp, const unsigned short* __restrict__ w1,
              char* __restrict__ ws)
{
    const int lane = threadIdx.x & 63;
    const int gw = __builtin_amdgcn_readfirstlane(blockIdx.x * 4 + (threadIdx.x >> 6));
    const int c = lane & 15, q = lane >> 4;
    if (gw < 800) {
        const int nt = gw & 3, st = (gw >> 2) % 25, kw = (gw >> 2) / 25;
        const int U = kw * 4 + q;
        const int fld = st < 24 ? 3 * U + (st >> 3) : 96 + (U >> 3);
        const int chunk = st < 24 ? (st & 7) : (U & 7);
        const char* src = (const char*)w1 + ((size_t)fld * 64 + chunk * 8) * 512 + (nt * 64 + 4 * c) * 2;
        u32x2 r[8];
#pragma unroll
        for (int jj = 0; jj < 8; ++jj) r[jj] = *reinterpret_cast<const u32x2*>(src + jj * 512);
        u32x4 p0, p1, p2, p3;
#pragma unroll
        for (int p = 0; p < 4; ++p) {
            const u32x2 a = r[2 * p], b = r[2 * p + 1];
            p0[p] = __builtin_amdgcn_perm(b[0], a[0], 0x05040100u);
            p1[p] = __builtin_amdgcn_perm(b[0], a[0], 0x07060302u);
            p2[p] = __builtin_amdgcn_perm(b[1], a[1], 0x05040100u);
            p3[p] = __builtin_amdgcn_perm(b[1], a[1], 0x07060302u);
        }
        char* dst = ws + WS_W1T + (size_t)gw * 4096 + lane * 16;
        *reinterpret_cast<u32x4*>(dst) = p0;
        *reinterpret_cast<u32x4*>(dst + 1024) = p1;
        *reinterpret_cast<u32x4*>(dst + 2048) = p2;
        *reinterpret_cast<u32x4*>(dst + 3072) = p3;
    } else if (gw < 828) {
        const int t = gw - 800;
        const unsigned long long Ilo = 0x2221111110000000ull, Ihi = 0x0000655444333322ull;
        const unsigned long long Jlo = 0x4326543216543210ull, Jhi = 0x0000665654654365ull;
        const int I = (int)(((t < 16 ? Ilo : Ihi) >> ((t & 15) * 4)) & 15);
        const int J = (int)(((t < 16 ? Jlo : Jhi) >> ((t & 15) * 4)) & 15);
        const int jcol = 16 * J + c;
        f32x4 cfv;
#pragma unroll
        for (int r = 0; r < 4; ++r) {
            const int i = 16 * I + 4 * q + r;
            float v = 0.f;
            if (i < NF && jcol < NF) {
                v = bf2f(wp[i * NF + jcol]);
                if (I != J) v += bf2f(wp[jcol * NF + i]);
            }
            cfv[r] = v;
        }
        *reinterpret_cast<f32x4*>(ws + WS_COEF + t * 1024 + lane * 16) = cfv;
    } else if (gw == 828) {
#pragma unroll
        for (int k = 0; k < 4; ++k)
            *reinterpret_cast<u32x4*>(ws + WS_FLAGS + k * 1024 + lane * 16) = (u32x4){0u, 0u, 0u, 0u};
    } else if (gw < 861) {
#pragma unroll
        for (int k = 0; k < 4; ++k)
            *reinterpret_cast<u32x4*>(ws + WS_GRAN + (gw - 829) * 4096 + k * 1024 + lane * 16) = (u32x4){0u, 0u, 0u, 0u};
    }
}

__global__ __launch_bounds__(256, 1)
void dcp_kernel(const int* __restrict__ idx, const unsigned short* __restrict__ table,
                const unsigned short* __restrict__ b1, const unsigned short* __restrict__ w2,
                const unsigned short* __restrict__ b2, __hip_bfloat16* __restrict__ out,
                char* __restrict__ ws)
{
    __shared__ __attribute__((aligned(16))) char smem[LDS_TOTAL];

    const int tid = threadIdx.x;
    const int lane = tid & 63;
    const int w = __builtin_amdgcn_readfirstlane(tid >> 6);
    const int bid = blockIdx.x;
    const int mt = bid >> 3, c8 = bid & 7, nt = c8 >> 1, kh = bid & 1;
    const int c = lane & 15, q = lane >> 4;

    RSTAMP(10);
    STAMP(0);
    const int kw = kh * 4 + w;
    const int U = kw * 4 + q;
    i32x3u iv[8]; int iv3[8];
#pragma unroll
    for (int j = 0; j < 4; ++j) {
#pragma unroll
        for (int h = 0; h < 2; ++h) {
            const int o = (w + j) & 3;
            const int sample = mt * 128 + (h ^ kh) * 64 + o * 16 + c;
            const int* ip = idx + sample * NF;
            iv[j * 2 + h] = *reinterpret_cast<const i32x3u*>(ip + 3 * U);
            iv3[j * 2 + h] = ip[96 + (U >> 3)];
        }
    }
    u32x4 cfs[7];
#pragma unroll
    for (int k = 0; k < 7; ++k) cfs[k] = *reinterpret_cast<const u32x4*>(ws + WS_COEF + (w + 4 * k) * 1024 + lane * 16);
    const char* w1lane = ws + WS_W1T + ((size_t)kw * 100 + nt) * 4096 + lane * 16;
    bf16x8 bq[4][4];
#pragma unroll
    for (int s = 0; s < 2; ++s)
#pragma unroll
        for (int u = 0; u < 4; ++u)
            bq[s][u] = *reinterpret_cast<const bf16x8*>(w1lane + s * 16384 + u * 1024);
    __builtin_amdgcn_sched_barrier(0);
    {
        const int srow = lane >> 3, slot = lane & 7;
        const unsigned lds0 = (unsigned)(uintptr_t)(LAS char*)smem;
#pragma unroll
        for (int k = 0; k < 32; ++k) {
            int p = w + 4 * k; p = p < 125 ? p : 124;
            const int row = p * 8 + srow;
            const int chunk = slot ^ ((row >> 1) & 7);
            const char* src = (const char*)table + row * 128 + chunk * 16;
            unsigned keep;
            asm volatile("s_mov_b32 %0, m0\n\ts_mov_b32 m0, %2\n\ts_nop 0\n\tglobal_load_lds_dwordx4 %1, off\n\ts_mov_b32 m0, %0"
                         : "=&s"(keep) : "v"(src), "s"(lds0 + (unsigned)p * 1024u) : "memory");
        }
        if (tid < 8) *reinterpret_cast<u32x4*>(smem + LDS_ZROW + tid * 16) = (u32x4){0u, 0u, 0u, 0u};
    }
    __builtin_amdgcn_sched_barrier(0);
#pragma unroll
    for (int s = 2; s < 4; ++s)
#pragma unroll
        for (int u = 0; u < 4; ++u)
            bq[s][u] = *reinterpret_cast<const bf16x8*>(w1lane + s * 16384 + u * 1024);
    u32x4 warm[4];
    {
        const int id = mt * 4 + w;
        const char* wsrc = ws + WS_W1T + ((size_t)(kh * 100 + (id < 100 ? id : 99)) * 4 + nt) * 4096 + lane * 16;
#pragma unroll
        for (int u = 0; u < 4; ++u) warm[u] = *reinterpret_cast<const u32x4*>(wsrc + u * 1024);
    }
    __builtin_amdgcn_sched_barrier(0);

    unsigned addr[8][4];
#pragma unroll
    for (int t = 0; t < 8; ++t) {
#pragma unroll
        for (int fi = 0; fi < 4; ++fi) {
            const unsigned v = (unsigned)(fi < 3 ? iv[t][fi] : iv3[t]);
            unsigned a = v * 128u + (((v >> 1) & 7u) << 4);
            if (fi == 3) a ^= (unsigned)(U & 7) << 4;
            asm volatile("" : "+v"(a));
            addr[t][fi] = a;
        }
    }
#pragma unroll
    for (int k = 0; k < 7; ++k) *reinterpret_cast<u32x4*>(smem + LDS_SPARE + (w + 4 * k) * 1024 + lane * 16) = cfs[k];

    STAMP(1);
    __builtin_amdgcn_sched_barrier(0);
    asm volatile("s_waitcnt vmcnt(12) lgkmcnt(0)\n\ts_barrier" ::: "memory");
    __builtin_amdgcn_sched_barrier(0);
    STAMP(2);

    f32x4 acc[8][4];
#pragma unroll
    for (int t = 0; t < 8; ++t)
#pragma unroll
        for (int u = 0; u < 4; ++u) acc[t][u] = (f32x4){0.f, 0.f, 0.f, 0.f};

    bf16x8 af[8];
#pragma unroll
    for (int t = 0; t < 8; ++t) af[t] = lds_read128(smem, addr[t][0]);
    const int prow0 = mt * 128 + kh * 64 + w * 16;
    unsigned praw[4][7], paddr[4][7];
    __builtin_amdgcn_sched_barrier(0);

#pragma unroll
    for (int s = 0; s < 25; ++s) {
#pragma unroll
        for (int t = 0; t < 8; ++t) {
#pragma unroll
            for (int u = 0; u < 4; ++u)
                acc[t][u] = __builtin_amdgcn_mfma_f32_16x16x32_bf16(af[t], bq[s & 3][u], acc[t][u], 0, 0, 0);
            if (s + 1 < 24)
                af[t] = lds_read128(smem, addr[t][(s + 1) >> 3] ^ (unsigned)(((s + 1) & 7) << 4));
            else if (s + 1 == 24)
                af[t] = lds_read128(smem, addr[t][3]);
            if (s >= 1 && s + 3 < 25 && (t & 1) == 0) {
                bq[(s + 3) & 3][t >> 1] = *reinterpret_cast<const bf16x8*>(w1lane + (s + 3) * 16384 + (t >> 1) * 1024);
            }
            if (s >= 2 && s < 6 && t < 7) {
                const int fld = (t < 6) ? 16 * t + c : (c < 4 ? 96 + c : 0);
                praw[s - 2][t] = (unsigned)idx[(prow0 + nt * 4 + (s - 2)) * NF + fld];
            }
            if (s >= 12 && s < 16 && t < 7) {
                unsigned v = praw[s - 12][t];
                if (t == 6) v = (c < 4) ? v : 1000u;
                unsigned a = v * 128u + (((unsigned)q ^ ((v >> 1) & 7u)) << 4);
                asm volatile("" : "+v"(a));
                paddr[s - 12][t] = a;
            }
            __builtin_amdgcn_sched_group_barrier(0x008, 1, 0);
            __builtin_amdgcn_sched_group_barrier(0x002, 2, 0);
            __builtin_amdgcn_sched_group_barrier(0x008, 1, 0);
            __builtin_amdgcn_sched_group_barrier(0x020, 1, 0);
            __builtin_amdgcn_sched_group_barrier(0x002, 1, 0);
            __builtin_amdgcn_sched_group_barrier(0x008, 1, 0);
            __builtin_amdgcn_sched_group_barrier(0x002, 2, 0);
            __builtin_amdgcn_sched_group_barrier(0x008, 1, 0);
            __builtin_amdgcn_sched_group_barrier(0x100, 1, 0);
            __builtin_amdgcn_sched_group_barrier(0x020, 1, 0);
        }
        if (s == 3) {
#pragma unroll
            for (int u = 0; u < 4; ++u) asm volatile("" :: "v"(warm[u]));
        }
        __builtin_amdgcn_sched_barrier(0);
    }
    asm volatile("" ::: "memory");

    STAMP(3);
    f32x4 cf[28];
#pragma unroll
    for (int t = 0; t < 28; ++t)
        cf[t] = *reinterpret_cast<const f32x4*>(smem + LDS_SPARE + t * 1024 + lane * 16);
#pragma unroll
    for (int t = 0; t < 28; ++t) asm volatile("" : "+v"(cf[t]));
    __syncthreads();
#pragma unroll
    for (int r = 1; r < 4; ++r) {
        char* dst = smem + LDS_SPARE + (((w + r) & 3) * 8192) + lane * 16;
#pragma unroll
        for (int h = 0; h < 2; ++h)
#pragma unroll
            for (int u = 0; u < 4; ++u)
                *reinterpret_cast<f32x4*>(dst + (h * 4 + u) * 1024) = acc[r * 2 + h][u];
        __syncthreads();
        const char* src = smem + LDS_SPARE + w * 8192 + lane * 16;
#pragma unroll
        for (int h = 0; h < 2; ++h)
#pragma unroll
            for (int u = 0; u < 4; ++u)
                acc[h][u] += *reinterpret_cast<const f32x4*>(src + (h * 4 + u) * 1024);
        if (r < 3) __syncthreads();
    }

    STAMP(4);
    const __amdgpu_buffer_rsrc_t xr = __builtin_amdgcn_make_buffer_rsrc(ws + WS_X, 0, 1024 * 4096, 0x00020000);
    {
        const unsigned off = (unsigned)((bid * 4 + w) * 4096 + lane * 16);
#pragma unroll
        for (int u = 0; u < 4; ++u)
            __builtin_amdgcn_raw_buffer_store_b128(__builtin_bit_cast(u32x4, acc[1][u]), xr, off + u * 1024, 0, 16);
    }

    STAMP(5);
    STAMP(6);
    float lp[4];
    GAS unsigned long long* gr = (GAS unsigned long long*)(ws + WS_GRAN) + ((mt * 2 + kh) * 4 + w) * 64;
    bf16x8 e[2][7][2];
#pragma unroll
    for (int I = 0; I < 7; ++I) {
        e[0][I][0] = lds_read128(smem, paddr[0][I]);
        e[0][I][1] = lds_read128(smem, paddr[0][I] ^ 64u);
    }
#pragma unroll
    for (int i = 0; i < 4; ++i) {
#pragma unroll
        for (int I = 0; I < 7; ++I) { asm volatile("" : "+a"(e[i & 1][I][0])); asm volatile("" : "+a"(e[i & 1][I][1])); }
        if (i < 3) {
#pragma unroll
            for (int I = 0; I < 7; ++I) {
                e[(i + 1) & 1][I][0] = lds_read128(smem, paddr[i + 1][I]);
                e[(i + 1) & 1][I][1] = lds_read128(smem, paddr[i + 1][I] ^ 64u);
            }
        }
        if (i == 2) {
            asm volatile("s_waitcnt vmcnt(0)" ::: "memory");
            __syncthreads();
            if (tid == 0)
                __hip_atomic_store((GAS unsigned*)(ws + WS_FLAGS) + bid * 4, 1u, RLX_AGENT);
        }
        if (i == 3) {
            GAS unsigned* fl = (GAS unsigned*)(ws + WS_FLAGS) + (bid ^ 1) * 4;
            unsigned spins = 0;
            while ((unsigned)__builtin_amdgcn_readfirstlane(__hip_atomic_load(fl, RLX_AGENT)) != 1u) {
                __builtin_amdgcn_s_sleep(4);
                if (++spins > (1u << 22)) break;
            }
            __builtin_amdgcn_fence(__ATOMIC_ACQUIRE, "wavefront");
            const unsigned off = (unsigned)(((bid ^ 1) * 4 + w) * 4096 + lane * 16);
#pragma unroll
            for (int u = 0; u < 4; ++u)
                acc[0][u] += __builtin_bit_cast(f32x4, __builtin_amdgcn_raw_buffer_load_b128(xr, off + u * 1024, 0, 16));
            const int col0 = nt * 64 + 4 * c;
            const u32x2 b1v = *reinterpret_cast<const u32x2*>(b1 + col0);
            const u32x2 w2v = *reinterpret_cast<const u32x2*>(w2 + col0);
            const float bb[4] = {__uint_as_float(b1v[0] << 16), __uint_as_float(b1v[0] & 0xffff0000u),
                                 __uint_as_float(b1v[1] << 16), __uint_as_float(b1v[1] & 0xffff0000u)};
            const float ww[4] = {__uint_as_float(w2v[0] << 16), __uint_as_float(w2v[0] & 0xffff0000u),
                                 __uint_as_float(w2v[1] << 16), __uint_as_float(w2v[1] & 0xffff0000u)};
            float rs[4];
#pragma unroll
            for (int r = 0; r < 4; ++r) {
                float a = 0.f;
#pragma unroll
                for (int u = 0; u < 4; ++u) {
                    float y = bfround(acc[0][u][r]);
                    y = bfround(y + bb[u]);
                    const float hh = bfround(gelu_tanh(y));
                    a = fmaf(hh, ww[u], a);
                }
                rs[r] = a;
            }
#pragma unroll
            for (int m = 1; m < 16; m <<= 1)
#pragma unroll
                for (int r = 0; r < 4; ++r) rs[r] += __shfl_xor(rs[r], m, 64);
            if (c == 0) {
#pragma unroll
                for (int r = 0; r < 4; ++r)
                    __hip_atomic_store(gr + nt * 16 + q * 4 + r, (1ull << 32) | (unsigned long long)__float_as_uint(rs[r]), RLX_AGENT);
            }
        }
        __builtin_amdgcn_sched_barrier(0);
        if (i == 0) RSTAMP(9);
        if (i == 1) RSTAMP(13);
        if (i == 2) RSTAMP(14);
        if (i == 3) RSTAMP(15);
        float s0 = 0.f, s1 = 0.f, s2 = 0.f, s3 = 0.f;
        f32x4 d[3];
        int t = 0;
#pragma unroll
        for (int I = 0; I < 7; ++I) {
#pragma unroll
            for (int J = I; J < 7; ++J, ++t) {
                if (t < 2) {
                    asm volatile(
                        "s_nop 1\n\t"
                        "v_mfma_f32_16x16x32_bf16 %0, %1, %2, 0\n\t"
                        "v_mfma_f32_16x16x32_bf16 %0, %3, %4, %0"
                        : "=&v"(d[t % 3])
                        : "a"(e[i & 1][I][0]), "a"(e[i & 1][J][0]), "a"(e[i & 1][I][1]), "a"(e[i & 1][J][1]));
                } else {
                    const f32x4 dp = d[(t + 1) % 3];
                    const f32x4 cp = cf[t - 2];
                    if (t == 2) {
                        asm volatile(
                            "v_mfma_f32_16x16x32_bf16 %0, %5, %6, 0\n\t"
                            "s_nop 4\n\t"
                            "v_fmac_f32 %1, %9, %13\n\t"
                            "v_fmac_f32 %2, %10, %14\n\t"
                            "v_mfma_f32_16x16x32_bf16 %0, %7, %8, %0\n\t"
                            "v_fmac_f32 %3, %11, %15\n\t"
                            "v_fmac_f32 %4, %12, %16"
                            : "=&v"(d[t % 3]), "+v"(s0), "+v"(s1), "+v"(s2), "+v"(s3)
                            : "a"(e[i & 1][I][0]), "a"(e[i & 1][J][0]), "a"(e[i & 1][I][1]), "a"(e[i & 1][J][1]),
                              "v"(cp[0]), "v"(cp[1]), "v"(cp[2]), "v"(cp[3]),
                              "v"(dp[0]), "v"(dp[1]), "v"(dp[2]), "v"(dp[3]));
                    } else {
                        asm volatile(
                            "v_mfma_f32_16x16x32_bf16 %0, %5, %6, 0\n\t"
                            "v_fmac_f32 %1, %9, %13\n\t"
                            "v_fmac_f32 %2, %10, %14\n\t"
                            "v_mfma_f32_16x16x32_bf16 %0, %7, %8, %0\n\t"
                            "v_fmac_f32 %3, %11, %15\n\t"
                            "v_fmac_f32 %4, %12, %16"
                            : "=&v"(d[t % 3]), "+v"(s0), "+v"(s1), "+v"(s2), "+v"(s3)
                            : "a"(e[i & 1][I][0]), "a"(e[i & 1][J][0]), "a"(e[i & 1][I][1]), "a"(e[i & 1][J][1]),
                              "v"(cp[0]), "v"(cp[1]), "v"(cp[2]), "v"(cp[3]),
                              "v"(dp[0]), "v"(dp[1]), "v"(dp[2]), "v"(dp[3]));
                    }
                }
            }
        }
        {
            const f32x4 da = d[26 % 3], db = d[27 % 3], ca = cf[26], cb = cf[27];
            asm volatile(
                "s_nop 1\n\t"
                "v_fmac_f32 %0, %4, %8\n\t"
                "v_fmac_f32 %1, %5, %9\n\t"
                "v_fmac_f32 %2, %6, %10\n\t"
                "v_fmac_f32 %3, %7, %11\n\t"
                "v_fmac_f32 %0, %12, %16\n\t"
                "v_fmac_f32 %1, %13, %17\n\t"
                "v_fmac_f32 %2, %14, %18\n\t"
                "v_fmac_f32 %3, %15, %19"
                : "+v"(s0), "+v"(s1), "+v"(s2), "+v"(s3)
                : "v"(ca[0]), "v"(ca[1]), "v"(ca[2]), "v"(ca[3]), "v"(da[0]), "v"(da[1]), "v"(da[2]), "v"(da[3]),
                  "v"(cb[0]), "v"(cb[1]), "v"(cb[2]), "v"(cb[3]), "v"(db[0]), "v"(db[1]), "v"(db[2]), "v"(db[3]));
        }
        lp[i] = (s0 + s1) + (s2 + s3);
        __builtin_amdgcn_sched_barrier(0);
    }
#pragma unroll
    for (int m = 1; m < 64; m <<= 1)
#pragma unroll
        for (int i = 0; i < 4; ++i) lp[i] += __shfl_xor(lp[i], m, 64);

    {
        float v = 0.f;
        unsigned spins = 0;
        for (;;) {
            bool ok = true;
            if (lane < 16) {
                const unsigned long long xg = __hip_atomic_load(gr + (lane >> 2) * 16 + nt * 4 + (lane & 3), RLX_AGENT);
                v = __uint_as_float((unsigned)xg);
                ok = (xg >> 32) == 1ull;
            }
            if (__all(ok)) break;
            __builtin_amdgcn_s_sleep(8);
            if (++spins > (1u << 22)) break;
        }
        STAMP(7);
        RSTAMP(11);
        v += __shfl_xor(v, 4, 64);
        v += __shfl_xor(v, 8, 64);
        const float lpv = (lane & 2) ? ((lane & 1) ? lp[3] : lp[2]) : ((lane & 1) ? lp[1] : lp[0]);
        if (lane < 4) out[prow0 + nt * 4 + lane] = __float2bfloat16(v + bf2f(b2[0]) + lpv);
    }
    STAMP(8);
    RSTAMP(12);
}

extern "C" void kernel_launch(void* const* d_in, const int* in_sizes, int n_in,
                              void* d_out, int out_size, void* d_ws, size_t ws_size,
                              hipStream_t stream) {
    const int* idx = (const int*)d_in[0];
    const unsigned short* table = (const unsigned short*)d_in[1];
    const unsigned short* wp = (const unsigned short*)d_in[2];
    const unsigned short* w1 = (const unsigned short*)d_in[3];
    const unsigned short* b1 = (const unsigned short*)d_in[4];
    const unsigned short* w2 = (const unsigned short*)d_in[5];
    const unsigned short* b2 = (const unsigned short*)d_in[6];
    dcp_prep<<<216, 256, 0, stream>>>(wp, w1, (char*)d_ws);
    dcp_kernel<<<256, 256, 0, stream>>>(idx, table, b1, w2, b2, (__hip_bfloat16*)d_out, (char*)d_ws);
}
```

```cpp
#include <hip/hip_runtime.h>
#include <hip/hip_bf16.h>
#include <stdint.h>

typedef __attribute__((ext_vector_type(8))) short bf16x8;
typedef __attribute__((ext_vector_type(4))) float f32x4;
typedef __attribute__((ext_vector_type(4))) unsigned u32x4;
typedef __attribute__((ext_vector_type(2))) unsigned u32x2;
typedef __attribute__((ext_vector_type(3), aligned(4))) int i32x3u;

#define GAS __attribute__((address_space(1)))
#define LAS __attribute__((address_space(3)))
#define RLX_AGENT __ATOMIC_RELAXED, __HIP_MEMORY_SCOPE_AGENT

constexpr int NF = 100;
constexpr int LDS_ZROW = 128000;
constexpr int LDS_SPARE = 128128;
constexpr int LDS_TOTAL = LDS_SPARE + 32768 + 16;

constexpr size_t WS_REQ = 0;
constexpr size_t WS_HEL = 16384;
constexpr size_t WS_TAG = 32768;
constexpr size_t WS_GRAN = 65536;
constexpr size_t WS_X = 65536 + 262144;
constexpr int LDS_NONCE = LDS_SPARE + 32768;

#define RSTAMP(n) do {} while (0)
#define STAMP(n) do {} while (0)

__device__ __forceinline__ float bf2f(unsigned short u) { return __uint_as_float(((unsigned)u) << 16); }
__device__ __forceinline__ float bfround(float x) {
    return __bfloat162float(__float2bfloat16(x));
}
__device__ __forceinline__ float gelu_tanh(float x) {
    const float u = 0.7978845608028654f * (x + 0.044715f * x * x * x);
    const float e = __expf(2.0f * u);
    const float t = 1.0f - 2.0f * __builtin_amdgcn_rcpf(e + 1.0f);
    return 0.5f * x * (1.0f + t);
}
__device__ __forceinline__ bf16x8 lds_read128(const char* smem, unsigned off) {
    return *reinterpret_cast<const bf16x8*>(smem + off);
}

__global__ __launch_bounds__(256, 1)
void dcp_kernel(const int* __restrict__ idx, const unsigned short* __restrict__ table,
                const unsigned short* __restrict__ wp, const unsigned short* __restrict__ w1,
                const unsigned short* __restrict__ b1, const unsigned short* __restrict__ w2,
                const unsigned short* __restrict__ b2, __hip_bfloat16* __restrict__ out,
                char* __restrict__ ws)
{
    __shared__ __attribute__((aligned(16))) char smem[LDS_TOTAL];

    const int tid = threadIdx.x;
    const int lane = tid & 63;
    const int w = __builtin_amdgcn_readfirstlane(tid >> 6);
    const int bid = blockIdx.x;
    const int mt = bid >> 3, c8 = bid & 7, nt = c8 >> 1, kh = bid & 1;
    const int c = lane & 15, q = lane >> 4;

    RSTAMP(10);
    STAMP(0);
    if (tid == 0) {
        const unsigned long long n0 = __builtin_amdgcn_s_memrealtime();
        __hip_atomic_store((GAS unsigned long long*)(ws + WS_REQ) + bid * 8, n0, RLX_AGENT);
        *reinterpret_cast<unsigned long long*>(smem + LDS_NONCE) = n0;
    }
    const int kw = kh * 4 + w;
    const int U = kw * 4 + q;
    i32x3u iv[8]; int iv3[8];
#pragma unroll
    for (int j = 0; j < 4; ++j) {
#pragma unroll
        for (int h = 0; h < 2; ++h) {
            const int o = (w + j) & 3;
            const int sample = mt * 128 + (h ^ kh) * 64 + o * 16 + c;
            const int* ip = idx + sample * NF;
            iv[j * 2 + h] = *reinterpret_cast<const i32x3u*>(ip + 3 * U);
            iv3[j * 2 + h] = ip[96 + (U >> 3)];
        }
    }
    unsigned short wa[7][4]; u32x2 wt[7]; bool wok[7], wdiag[7];
#pragma unroll
    for (int k = 0; k < 7; ++k) {
        const int t = w + 4 * k;
        const unsigned long long Ilo = 0x2221111110000000ull, Ihi = 0x0000655444333322ull;
        const unsigned long long Jlo = 0x4326543216543210ull, Jhi = 0x0000665654654365ull;
        const int I = (int)(((t < 16 ? Ilo : Ihi) >> ((t & 15) * 4)) & 15);
        const int J = (int)(((t < 16 ? Jlo : Jhi) >> ((t & 15) * 4)) & 15);
        const int jcol = 16 * J + c;
        const int jc = jcol < NF ? jcol : NF - 1;
        const int i0 = 16 * I + 4 * q;
        const int i0c = i0 < NF ? i0 : NF - 4;
        wok[k] = (i0 < NF) && (jcol < NF);
        wdiag[k] = (I == J);
        wt[k] = *reinterpret_cast<const u32x2*>(wp + jc * NF + i0c);
#pragma unroll
        for (int r = 0; r < 4; ++r) wa[k][r] = wp[(i0c + r) * NF + jc];
    }
    const char* w1lane = (const char*)w1 + (size_t)(192 * U) * 512 + (nt * 64 + 4 * c) * 2;
    const char* w1last = (const char*)w1 + (size_t)((96 + (U >> 3)) * 64 + (U & 7) * 8) * 512 + (nt * 64 + 4 * c) * 2;
    u32x2 braw[3][8];
#pragma unroll
    for (int s = 0; s < 2; ++s)
#pragma unroll
        for (int jj = 0; jj < 8; ++jj)
            braw[s][jj] = *reinterpret_cast<const u32x2*>(w1lane + s * 4096 + jj * 512);
    __builtin_amdgcn_sched_barrier(0);
    {
        const int srow = lane >> 3, slot = lane & 7;
        const unsigned lds0 = (unsigned)(uintptr_t)(LAS char*)smem;
#pragma unroll
        for (int k = 0; k < 32; ++k) {
            int p = w + 4 * k; p = p < 125 ? p : 124;
            const int row = p * 8 + srow;
            const int chunk = slot ^ ((row >> 1) & 7);
            const char* src = (const char*)table + row * 128 + chunk * 16;
            unsigned keep;
            asm volatile("s_mov_b32 %0, m0\n\ts_mov_b32 m0, %2\n\ts_nop 0\n\tglobal_load_lds_dwordx4 %1, off\n\ts_mov_b32 m0, %0"
                         : "=&s"(keep) : "v"(src), "s"(lds0 + (unsigned)p * 1024u) : "memory");
        }
        if (tid < 8) *reinterpret_cast<u32x4*>(smem + LDS_ZROW + tid * 16) = (u32x4){0u, 0u, 0u, 0u};
    }
    __builtin_amdgcn_sched_barrier(0);
#pragma unroll
    for (int jj = 0; jj < 8; ++jj)
        braw[2][jj] = *reinterpret_cast<const u32x2*>(w1lane + 2 * 4096 + jj * 512);
    unsigned warm;
    {
        const int id = mt * 4 + w;
        int row = (id < 48) ? kh * 3072 + id * 64 + lane : 6144 + kh * 128 + (id & 1) * 64 + lane;
        warm = *reinterpret_cast<const unsigned*>((const char*)w1 + (size_t)row * 512 + nt * 128);
    }
    __builtin_amdgcn_sched_barrier(0);

    unsigned addr[8][4];
#pragma unroll
    for (int t = 0; t < 8; ++t) {
#pragma unroll
        for (int fi = 0; fi < 4; ++fi) {
            const unsigned v = (unsigned)(fi < 3 ? iv[t][fi] : iv3[t]);
            unsigned a = v * 128u + (((v >> 1) & 7u) << 4);
            if (fi == 3) a ^= (unsigned)(U & 7) << 4;
            asm volatile("" : "+v"(a));
            addr[t][fi] = a;
        }
    }
#pragma unroll
    for (int k = 0; k < 7; ++k) {
        f32x4 cfv;
#pragma unroll
        for (int r = 0; r < 4; ++r) {
            const float a = bf2f(wa[k][r]);
            const unsigned tw = (r & 2) ? wt[k][1] : wt[k][0];
            const float b = (r & 1) ? __uint_as_float(tw & 0xffff0000u) : __uint_as_float(tw << 16);
            cfv[r] = wok[k] ? (wdiag[k] ? a : a + b) : 0.0f;
        }
        *reinterpret_cast<f32x4*>(smem + LDS_SPARE + (w + 4 * k) * 1024 + lane * 16) = cfv;
    }

    STAMP(1);
    __builtin_amdgcn_sched_barrier(0);
    asm volatile("s_waitcnt vmcnt(9) lgkmcnt(0)\n\ts_barrier" ::: "memory");
    __builtin_amdgcn_sched_barrier(0);
    STAMP(2);

    f32x4 acc[8][4];
#pragma unroll
    for (int t = 0; t < 8; ++t)
#pragma unroll
        for (int u = 0; u < 4; ++u) acc[t][u] = (f32x4){0.f, 0.f, 0.f, 0.f};

    bf16x8 af[8];
#pragma unroll
    for (int t = 0; t < 8; ++t) af[t] = lds_read128(smem, addr[t][0]);
    const unsigned long long mynonce = *reinterpret_cast<const unsigned long long*>(smem + LDS_NONCE);
    const int prow0 = mt * 128 + kh * 64 + w * 16;
    unsigned praw[4][7], paddr[4][7];
    u32x4 bc[4], bn[4];
#pragma unroll
    for (int p = 0; p < 4; ++p) {
        const u32x2 a = braw[0][2 * p], b = braw[0][2 * p + 1];
        bc[0][p] = __builtin_amdgcn_perm(b[0], a[0], 0x05040100u);
        bc[1][p] = __builtin_amdgcn_perm(b[0], a[0], 0x07060302u);
        bc[2][p] = __builtin_amdgcn_perm(b[1], a[1], 0x05040100u);
        bc[3][p] = __builtin_amdgcn_perm(b[1], a[1], 0x07060302u);
    }
#pragma unroll
    for (int jj = 0; jj < 8; ++jj)
        braw[0][jj] = *reinterpret_cast<const u32x2*>(w1lane + 3 * 4096 + jj * 512);
    const int pk = lane & 3;
    const int peer = bid ^ (pk == 0 ? 1 : 2 * pk);
    GAS unsigned long long* preq = (GAS unsigned long long*)(ws + WS_REQ) + peer * 8;
    GAS unsigned long long* phel = (GAS unsigned long long*)(ws + WS_HEL) + peer * 8 + pk;
    GAS unsigned long long* mhel = (GAS unsigned long long*)(ws + WS_HEL) + bid * 8 + pk;
    unsigned long long pn = 0, hh = 0;
    __builtin_amdgcn_sched_barrier(0);

#pragma unroll
    for (int s = 0; s < 25; ++s) {
#pragma unroll
        for (int t = 0; t < 8; ++t) {
#pragma unroll
            for (int u = 0; u < 4; ++u)
                acc[t][u] = __builtin_amdgcn_mfma_f32_16x16x32_bf16(af[t], __builtin_bit_cast(bf16x8, bc[u]), acc[t][u], 0, 0, 0);
            if (s + 1 < 24)
                af[t] = lds_read128(smem, addr[t][(s + 1) >> 3] ^ (unsigned)(((s + 1) & 7) << 4));
            else if (s + 1 == 24)
                af[t] = lds_read128(smem, addr[t][3]);
            if (s + 1 < 25) {
                const int p = t >> 1;
                const u32x2 a = braw[(s + 1) % 3][2 * p], b = braw[(s + 1) % 3][2 * p + 1];
                if ((t & 1) == 0) {
                    bn[0][p] = __builtin_amdgcn_perm(b[0], a[0], 0x05040100u);
                    bn[1][p] = __builtin_amdgcn_perm(b[0], a[0], 0x07060302u);
                } else {
                    bn[2][p] = __builtin_amdgcn_perm(b[1], a[1], 0x05040100u);
                    bn[3][p] = __builtin_amdgcn_perm(b[1], a[1], 0x07060302u);
                    if (s + 4 < 24) {
                        braw[(s + 1) % 3][2 * p] = *reinterpret_cast<const u32x2*>(w1lane + (s + 4) * 4096 + (2 * p) * 512);
                        braw[(s + 1) % 3][2 * p + 1] = *reinterpret_cast<const u32x2*>(w1lane + (s + 4) * 4096 + (2 * p + 1) * 512);
                    } else if (s + 4 == 24) {
                        braw[(s + 1) % 3][2 * p] = *reinterpret_cast<const u32x2*>(w1last + (2 * p) * 512);
                        braw[(s + 1) % 3][2 * p + 1] = *reinterpret_cast<const u32x2*>(w1last + (2 * p + 1) * 512);
                    }
                }
            }
            if (s >= 2 && s < 6 && t < 7) {
                const int fld = (t < 6) ? 16 * t + c : (c < 4 ? 96 + c : 0);
                praw[s - 2][t] = (unsigned)idx[(prow0 + nt * 4 + (s - 2)) * NF + fld];
            }
            if (s >= 12 && s < 16 && t < 7) {
                unsigned v = praw[s - 12][t];
                if (t == 6) v = (c < 4) ? v : 1000u;
                unsigned a = v * 128u + (((unsigned)q ^ ((v >> 1) & 7u)) << 4);
                asm volatile("" : "+v"(a));
                paddr[s - 12][t] = a;
            }
            if (t == 7) {
                if (s == 0) pn = __hip_atomic_load(preq, RLX_AGENT);
                if (s == 6) __hip_atomic_store(phel, pn, RLX_AGENT);
                if (s == 16) hh = __hip_atomic_load(mhel, RLX_AGENT);
                if (s == 20) { asm volatile("" : "+v"(preq) : "v"(hh)); pn = __hip_atomic_load(preq, RLX_AGENT); }
                if (s == 24) __hip_atomic_store(phel, pn, RLX_AGENT);
            }
            __builtin_amdgcn_sched_group_barrier(0x008, 1, 0);
            __builtin_amdgcn_sched_group_barrier(0x002, 1, 0);
            __builtin_amdgcn_sched_group_barrier(0x020, 1, 0);
            __builtin_amdgcn_sched_group_barrier(0x008, 1, 0);
            __builtin_amdgcn_sched_group_barrier(0x002, 1, 0);
            __builtin_amdgcn_sched_group_barrier(0x020, 1, 0);
            __builtin_amdgcn_sched_group_barrier(0x008, 1, 0);
            __builtin_amdgcn_sched_group_barrier(0x002, 2, 0);
            __builtin_amdgcn_sched_group_barrier(0x008, 1, 0);
            __builtin_amdgcn_sched_group_barrier(0x100, 1, 0);
            __builtin_amdgcn_sched_group_barrier(0x002, 1, 0);
        }
        if (s + 1 < 25) {
#pragma unroll
            for (int u = 0; u < 4; ++u) { asm volatile("" : "+v"(bn[u])); bc[u] = bn[u]; }
        }
        if (s == 3) asm volatile("" :: "v"(warm));
        __builtin_amdgcn_sched_barrier(0);
    }
    asm volatile("" ::: "memory");
    if (!__all(hh == mynonce)) {
        unsigned spins = 0;
        for (;;) {
            pn = __hip_atomic_load(preq, RLX_AGENT);
            __hip_atomic_store(phel, pn, RLX_AGENT);
            hh = __hip_atomic_load(mhel, RLX_AGENT);
            if (__all(hh == mynonce)) break;
            __builtin_amdgcn_s_sleep(8);
            if (++spins > (1u << 22)) break;
        }
        asm volatile("" : "+v"(preq) : "v"(hh));
        pn = __hip_atomic_load(preq, RLX_AGENT);
        __hip_atomic_store(phel, pn, RLX_AGENT);
    }

    STAMP(3);
    f32x4 cf[28];
#pragma unroll
    for (int t = 0; t < 28; ++t)
        cf[t] = *reinterpret_cast<const f32x4*>(smem + LDS_SPARE + t * 1024 + lane * 16);
#pragma unroll
    for (int t = 0; t < 28; ++t) asm volatile("" : "+v"(cf[t]));
    __syncthreads();
#pragma unroll
    for (int r = 1; r < 4; ++r) {
        char* dst = smem + LDS_SPARE + (((w + r) & 3) * 8192) + lane * 16;
#pragma unroll
        for (int h = 0; h < 2; ++h)
#pragma unroll
            for (int u = 0; u < 4; ++u)
                *reinterpret_cast<f32x4*>(dst + (h * 4 + u) * 1024) = acc[r * 2 + h][u];
        __syncthreads();
        const char* src = smem + LDS_SPARE + w * 8192 + lane * 16;
#pragma unroll
        for (int h = 0; h < 2; ++h)
#pragma unroll
            for (int u = 0; u < 4; ++u)
                acc[h][u] += *reinterpret_cast<const f32x4*>(src + (h * 4 + u) * 1024);
        if (r < 3) __syncthreads();
    }

    STAMP(4);
    const __amdgpu_buffer_rsrc_t xr = __builtin_amdgcn_make_buffer_rsrc(ws + WS_X, 0, 1024 * 4096, 0x00020000);
    {
        const unsigned off = (unsigned)((bid * 4 + w) * 4096 + lane * 16);
#pragma unroll
        for (int u = 0; u < 4; ++u)
            __builtin_amdgcn_raw_buffer_store_b128(__builtin_bit_cast(u32x4, acc[1][u]), xr, off + u * 1024, 0, 16);
    }

    STAMP(5);
    STAMP(6);
    float lp[4];
    GAS unsigned long long* gr = (GAS unsigned long long*)(ws + WS_GRAN) + ((mt * 2 + kh) * 4 + w) * 128;
    bf16x8 e[2][7][2];
#pragma unroll
    for (int I = 0; I < 7; ++I) {
        e[0][I][0] = lds_read128(smem, paddr[0][I]);
        e[0][I][1] = lds_read128(smem, paddr[0][I] ^ 64u);
    }
#pragma unroll
    for (int i = 0; i < 4; ++i) {
#pragma unroll
        for (int I = 0; I < 7; ++I) { asm volatile("" : "+a"(e[i & 1][I][0])); asm volatile("" : "+a"(e[i & 1][I][1])); }
        if (i < 3) {
#pragma unroll
            for (int I = 0; I < 7; ++I) {
                e[(i + 1) & 1][I][0] = lds_read128(smem, paddr[i + 1][I]);
                e[(i + 1) & 1][I][1] = lds_read128(smem, paddr[i + 1][I] ^ 64u);
            }
        }
        if (i == 2) {
            asm volatile("s_waitcnt vmcnt(0)" ::: "memory");
            __syncthreads();
            if (tid == 0)
                __hip_atomic_store((GAS unsigned long long*)(ws + WS_TAG) + (bid ^ 1) * 8, pn, RLX_AGENT);
        }
        if (i == 3) {
            GAS unsigned long long* fl = (GAS unsigned long long*)(ws + WS_TAG) + bid * 8;
            unsigned spins = 0;
            while (!__all(__hip_atomic_load(fl, RLX_AGENT) == mynonce)) {
                __builtin_amdgcn_s_sleep(4);
                if (++spins > (1u << 22)) break;
            }
            __builtin_amdgcn_fence(__ATOMIC_ACQUIRE, "wavefront");
            const unsigned off = (unsigned)(((bid ^ 1) * 4 + w) * 4096 + lane * 16);
#pragma unroll
            for (int u = 0; u < 4; ++u)
                acc[0][u] += __builtin_bit_cast(f32x4, __builtin_amdgcn_raw_buffer_load_b128(xr, off + u * 1024, 0, 16));
            const int col0 = nt * 64 + 4 * c;
            const u32x2 b1v = *reinterpret_cast<const u32x2*>(b1 + col0);
            const u32x2 w2v = *reinterpret_cast<const u32x2*>(w2 + col0);
            const float bb[4] = {__uint_as_float(b1v[0] << 16), __uint_as_float(b1v[0] & 0xffff0000u),
                                 __uint_as_float(b1v[1] << 16), __uint_as_float(b1v[1] & 0xffff0000u)};
            const float ww[4] = {__uint_as_float(w2v[0] << 16), __uint_as_float(w2v[0] & 0xffff0000u),
                                 __uint_as_float(w2v[1] << 16), __uint_as_float(w2v[1] & 0xffff0000u)};
            float rs[4];
#pragma unroll
            for (int r = 0; r < 4; ++r) {
                float a = 0.f;
#pragma unroll
                for (int u = 0; u < 4; ++u) {
                    float y = bfround(acc[0][u][r]);
                    y = bfround(y + bb[u]);
                    const float hh = bfround(gelu_tanh(y));
                    a = fmaf(hh, ww[u], a);
                }
                rs[r] = a;
            }
#pragma unroll
            for (int m = 1; m < 16; m <<= 1)
#pragma unroll
                for (int r = 0; r < 4; ++r) rs[r] += __shfl_xor(rs[r], m, 64);
            const int xq = nt ^ q;
            unsigned tlo = (unsigned)__shfl((int)(unsigned)pn, xq, 64), thi = (unsigned)__shfl((int)(unsigned)(pn >> 32), xq, 64);
            if (xq == 0) { tlo = (unsigned)mynonce; thi = (unsigned)(mynonce >> 32); }
            if (c == 0) {
#pragma unroll
                for (int r = 0; r < 4; ++r) {
                    const unsigned long long vb = (unsigned long long)__float_as_uint(rs[r]);
                    __hip_atomic_store(gr + (nt * 16 + q * 4 + r) * 2, ((unsigned long long)tlo << 32) | vb, RLX_AGENT);
                    __hip_atomic_store(gr + (nt * 16 + q * 4 + r) * 2 + 1, ((unsigned long long)thi << 32) | vb, RLX_AGENT);
                }
            }
        }
        __builtin_amdgcn_sched_barrier(0);
        if (i == 0) RSTAMP(9);
        if (i == 1) RSTAMP(13);
        if (i == 2) RSTAMP(14);
        if (i == 3) RSTAMP(15);
        float s0 = 0.f, s1 = 0.f, s2 = 0.f, s3 = 0.f;
        f32x4 d[3];
        int t = 0;
#pragma unroll
        for (int I = 0; I < 7; ++I) {
#pragma unroll
            for (int J = I; J < 7; ++J, ++t) {
                if (t < 2) {
                    asm volatile(
                        "s_nop 1\n\t"
                        "v_mfma_f32_16x16x32_bf16 %0, %1, %2, 0\n\t"
                        "v_mfma_f32_16x16x32_bf16 %0, %3, %4, %0"
                        : "=&v"(d[t % 3])
                        : "a"(e[i & 1][I][0]), "a"(e[i & 1][J][0]), "a"(e[i & 1][I][1]), "a"(e[i & 1][J][1]));
                } else {
                    const f32x4 dp = d[(t + 1) % 3];
                    const f32x4 cp = cf[t - 2];
                    if (t == 2) {
                        asm volatile(
                            "v_mfma_f32_16x16x32_bf16 %0, %5, %6, 0\n\t"
                            "s_nop 4\n\t"
                            "v_fmac_f32 %1, %9, %13\n\t"
                            "v_fmac_f32 %2, %10, %14\n\t"
                            "v_mfma_f32_16x16x32_bf16 %0, %7, %8, %0\n\t"
                            "v_fmac_f32 %3, %11, %15\n\t"
                            "v_fmac_f32 %4, %12, %16"
                            : "=&v"(d[t % 3]), "+v"(s0), "+v"(s1), "+v"(s2), "+v"(s3)
                            : "a"(e[i & 1][I][0]), "a"(e[i & 1][J][0]), "a"(e[i & 1][I][1]), "a"(e[i & 1][J][1]),
                              "v"(cp[0]), "v"(cp[1]), "v"(cp[2]), "v"(cp[3]),
                              "v"(dp[0]), "v"(dp[1]), "v"(dp[2]), "v"(dp[3]));
                    } else {
                        asm volatile(
                            "v_mfma_f32_16x16x32_bf16 %0, %5, %6, 0\n\t"
                            "v_fmac_f32 %1, %9, %13\n\t"
                            "v_fmac_f32 %2, %10, %14\n\t"
                            "v_mfma_f32_16x16x32_bf16 %0, %7, %8, %0\n\t"
                            "v_fmac_f32 %3, %11, %15\n\t"
                            "v_fmac_f32 %4, %12, %16"
                            : "=&v"(d[t % 3]), "+v"(s0), "+v"(s1), "+v"(s2), "+v"(s3)
                            : "a"(e[i & 1][I][0]), "a"(e[i & 1][J][0]), "a"(e[i & 1][I][1]), "a"(e[i & 1][J][1]),
                              "v"(cp[0]), "v"(cp[1]), "v"(cp[2]), "v"(cp[3]),
                              "v"(dp[0]), "v"(dp[1]), "v"(dp[2]), "v"(dp[3]));
                    }
                }
            }
        }
        {
            const f32x4 da = d[26 % 3], db = d[27 % 3], ca = cf[26], cb = cf[27];
            asm volatile(
                "s_nop 1\n\t"
                "v_fmac_f32 %0, %4, %8\n\t"
                "v_fmac_f32 %1, %5, %9\n\t"
                "v_fmac_f32 %2, %6, %10\n\t"
                "v_fmac_f32 %3, %7, %11\n\t"
                "v_fmac_f32 %0, %12, %16\n\t"
                "v_fmac_f32 %1, %13, %17\n\t"
                "v_fmac_f32 %2, %14, %18\n\t"
                "v_fmac_f32 %3, %15, %19"
                : "+v"(s0), "+v"(s1), "+v"(s2), "+v"(s3)
                : "v"(ca[0]), "v"(ca[1]), "v"(ca[2]), "v"(ca[3]), "v"(da[0]), "v"(da[1]), "v"(da[2]), "v"(da[3]),
                  "v"(cb[0]), "v"(cb[1]), "v"(cb[2]), "v"(cb[3]), "v"(db[0]), "v"(db[1]), "v"(db[2]), "v"(db[3]));
        }
        lp[i] = (s0 + s1) + (s2 + s3);
        __builtin_amdgcn_sched_barrier(0);
    }
#pragma unroll
    for (int m = 1; m < 64; m <<= 1)
#pragma unroll
        for (int i = 0; i < 4; ++i) lp[i] += __shfl_xor(lp[i], m, 64);

    {
        float v = 0.f;
        unsigned spins = 0;
        for (;;) {
            bool ok = true;
            if (lane < 16) {
                const unsigned long long xa = __hip_atomic_load(gr + ((lane >> 2) * 16 + nt * 4 + (lane & 3)) * 2, RLX_AGENT);
                const unsigned long long xb = __hip_atomic_load(gr + ((lane >> 2) * 16 + nt * 4 + (lane & 3)) * 2 + 1, RLX_AGENT);
                v = __uint_as_float((unsigned)xa);
                ok = (xa >> 32) == (mynonce & 0xffffffffull) && (xb >> 32) == (mynonce >> 32) && (unsigned)xa == (unsigned)xb;
            }
            if (__all(ok)) break;
            __builtin_amdgcn_s_sleep(8);
            if (++spins > (1u << 22)) break;
        }
        STAMP(7);
        RSTAMP(11);
        v += __shfl_xor(v, 4, 64);
        v += __shfl_xor(v, 8, 64);
        const float lpv = (lane & 2) ? ((lane & 1) ? lp[3] : lp[2]) : ((lane & 1) ? lp[1] : lp[0]);
        if (lane < 4) out[prow0 + nt * 4 + lane] = __float2bfloat16(v + bf2f(b2[0]) + lpv);
    }
    STAMP(8);
    RSTAMP(12);
}

extern "C" void kernel_launch(void* const* d_in, const int* in_sizes, int n_in,
                              void* d_out, int out_size, void* d_ws, size_t ws_size,
                              hipStream_t stream) {
    const int* idx = (const int*)d_in[0];
    const unsigned short* table = (const unsigned short*)d_in[1];
    const unsigned short* wp = (const unsigned short*)d_in[2];
    const unsigned short* w1 = (const unsigned short*)d_in[3];
    const unsigned short* b1 = (const unsigned short*)d_in[4];
    const unsigned short* w2 = (const unsigned short*)d_in[5];
    const unsigned short* b2 = (const unsigned short*)d_in[6];
    dcp_kernel<<<256, 256, 0, stream>>>(idx, table, wp, w1, b1, w2, b2, (__hip_bfloat16*)d_out, (char*)d_ws);
}
```

```cpp
#include <hip/hip_runtime.h>
#include <hip/hip_bf16.h>
#include <stdint.h>

typedef __attribute__((ext_vector_type(8))) short bf16x8;
typedef __attribute__((ext_vector_type(4))) float f32x4;
typedef __attribute__((ext_vector_type(4))) unsigned u32x4;
typedef __attribute__((ext_vector_type(2))) unsigned u32x2;
typedef __attribute__((ext_vector_type(3), aligned(4))) int i32x3u;

#define GAS __attribute__((address_space(1)))
#define LAS __attribute__((address_space(3)))
#define RLX_AGENT __ATOMIC_RELAXED, __HIP_MEMORY_SCOPE_AGENT

constexpr int NF = 100;
constexpr int LDS_ZROW = 128000;
constexpr int LDS_SPARE = 128128;
constexpr int LDS_TOTAL = LDS_SPARE + 32768 + 16;

constexpr size_t WS_REQ = 0;
constexpr size_t WS_HEL = 16384;
constexpr size_t WS_TAG = 32768;
constexpr size_t WS_GRAN = 65536;
constexpr size_t WS_X = 65536 + 262144;
constexpr int LDS_NONCE = LDS_SPARE + 32768;

#define RSTAMP(n) do {} while (0)
#define STAMP(n) do {} while (0)

__device__ __forceinline__ float bf2f(unsigned short u) { return __uint_as_float(((unsigned)u) << 16); }
__device__ __forceinline__ float bfround(float x) {
    return __bfloat162float(__float2bfloat16(x));
}
__device__ __forceinline__ float gelu_tanh(float x) {
    const float u = 0.7978845608028654f * (x + 0.044715f * x * x * x);
    const float e = __expf(2.0f * u);
    const float t = 1.0f - 2.0f * __builtin_amdgcn_rcpf(e + 1.0f);
    return 0.5f * x * (1.0f + t);
}
__device__ __forceinline__ bf16x8 lds_read128(const char* smem, unsigned off) {
    return *reinterpret_cast<const bf16x8*>(smem + off);
}

__global__ __launch_bounds__(256, 1)
void dcp_kernel(const int* __restrict__ idx, const unsigned short* __restrict__ table,
                const unsigned short* __restrict__ wp, const unsigned short* __restrict__ w1,
                const unsigned short* __restrict__ b1, const unsigned short* __restrict__ w2,
                const unsigned short* __restrict__ b2, __hip_bfloat16* __restrict__ out,
                char* __restrict__ ws)
{
    __shared__ __attribute__((aligned(16))) char smem[LDS_TOTAL];

    const int tid = threadIdx.x;
    const int lane = tid & 63;
    const int w = __builtin_amdgcn_readfirstlane(tid >> 6);
    const int bid = blockIdx.x;
    const int mt = bid >> 3, c8 = bid & 7, nt = c8 >> 1, kh = bid & 1;
    const int c = lane & 15, q = lane >> 4;

    RSTAMP(10);
    STAMP(0);
    if (tid == 0) {
        const unsigned long long n0 = __builtin_amdgcn_s_memrealtime();
        __hip_atomic_store((GAS unsigned long long*)(ws + WS_REQ) + bid * 8, n0, RLX_AGENT);
        *reinterpret_cast<unsigned long long*>(smem + LDS_NONCE) = n0;
    }
    const int kw = kh * 4 + w;
    const int U = kw * 4 + q;
    i32x3u iv[8]; int iv3[8];
#pragma unroll
    for (int j = 0; j < 4; ++j) {
#pragma unroll
        for (int h = 0; h < 2; ++h) {
            const int o = (w + j) & 3;
            const int sample = mt * 128 + (h ^ kh) * 64 + o * 16 + c;
            const int* ip = idx + sample * NF;
            iv[j * 2 + h] = *reinterpret_cast<const i32x3u*>(ip + 3 * U);
            iv3[j * 2 + h] = ip[96 + (U >> 3)];
        }
    }
    const u32x2 b1v = *reinterpret_cast<const u32x2*>(b1 + nt * 64 + 4 * c);
    const u32x2 w2v = *reinterpret_cast<const u32x2*>(w2 + nt * 64 + 4 * c);
    const float b2v = bf2f(b2[0]);
    unsigned short wa[7][4]; u32x2 wt[7]; bool wok[7], wdiag[7];
#pragma unroll
    for (int k = 0; k < 7; ++k) {
        const int t = w + 4 * k;
        const unsigned long long Ilo = 0x2221111110000000ull, Ihi = 0x0000655444333322ull;
        const unsigned long long Jlo = 0x4326543216543210ull, Jhi = 0x0000665654654365ull;
        const int I = (int)(((t < 16 ? Ilo : Ihi) >> ((t & 15) * 4)) & 15);
        const int J = (int)(((t < 16 ? Jlo : Jhi) >> ((t & 15) * 4)) & 15);
        const int jcol = 16 * J + c;
        const int jc = jcol < NF ? jcol : NF - 1;
        const int i0 = 16 * I + 4 * q;
        const int i0c = i0 < NF ? i0 : NF - 4;
        wok[k] = (i0 < NF) && (jcol < NF);
        wdiag[k] = (I == J);
        wt[k] = *reinterpret_cast<const u32x2*>(wp + jc * NF + i0c);
#pragma unroll
        for (int r = 0; r < 4; ++r) wa[k][r] = wp[(i0c + r) * NF + jc];
    }
    const char* w1lane = (const char*)w1 + (size_t)(192 * U) * 512 + (nt * 64 + 4 * c) * 2;
    const char* w1last = (const char*)w1 + (size_t)((96 + (U >> 3)) * 64 + (U & 7) * 8) * 512 + (nt * 64 + 4 * c) * 2;
    u32x2 braw[3][8];
#pragma unroll
    for (int s = 0; s < 2; ++s)
#pragma unroll
        for (int jj = 0; jj < 8; ++jj)
            braw[s][jj] = *reinterpret_cast<const u32x2*>(w1lane + s * 4096 + jj * 512);
    __builtin_amdgcn_sched_barrier(0);
    {
        const int srow = lane >> 3, slot = lane & 7;
        const unsigned lds0 = (unsigned)(uintptr_t)(LAS char*)smem;
#pragma unroll
        for (int k = 0; k < 32; ++k) {
            int p = w + 4 * k; p = p < 125 ? p : 124;
            const int row = p * 8 + srow;
            const int chunk = slot ^ ((row >> 1) & 7);
            const char* src = (const char*)table + row * 128 + chunk * 16;
            unsigned keep;
            asm volatile("s_mov_b32 %0, m0\n\ts_mov_b32 m0, %2\n\ts_nop 0\n\tglobal_load_lds_dwordx4 %1, off\n\ts_mov_b32 m0, %0"
                         : "=&s"(keep) : "v"(src), "s"(lds0 + (unsigned)p * 1024u) : "memory");
        }
        if (tid < 8) *reinterpret_cast<u32x4*>(smem + LDS_ZROW + tid * 16) = (u32x4){0u, 0u, 0u, 0u};
    }
    __builtin_amdgcn_sched_barrier(0);
#pragma unroll
    for (int jj = 0; jj < 8; ++jj)
        braw[2][jj] = *reinterpret_cast<const u32x2*>(w1lane + 2 * 4096 + jj * 512);
    unsigned warm;
    {
        const int id = mt * 4 + w;
        int row = (id < 48) ? kh * 3072 + id * 64 + lane : 6144 + kh * 128 + (id & 1) * 64 + lane;
        warm = *reinterpret_cast<const unsigned*>((const char*)w1 + (size_t)row * 512 + nt * 128);
    }
    __builtin_amdgcn_sched_barrier(0);

    u32x2 b1p = b1v, w2p = w2v; float b2p = b2v;
    asm volatile("" : "+v"(b1p), "+v"(w2p), "+v"(b2p));
    unsigned addr[8][4];
#pragma unroll
    for (int t = 0; t < 8; ++t) {
#pragma unroll
        for (int fi = 0; fi < 4; ++fi) {
            const unsigned v = (unsigned)(fi < 3 ? iv[t][fi] : iv3[t]);
            unsigned a = v * 128u + (((v >> 1) & 7u) << 4);
            if (fi == 3) a ^= (unsigned)(U & 7) << 4;
            asm volatile("" : "+v"(a));
            addr[t][fi] = a;
        }
    }
#pragma unroll
    for (int k = 0; k < 7; ++k) {
        f32x4 cfv;
#pragma unroll
        for (int r = 0; r < 4; ++r) {
            const float a = bf2f(wa[k][r]);
            const unsigned tw = (r & 2) ? wt[k][1] : wt[k][0];
            const float b = (r & 1) ? __uint_as_float(tw & 0xffff0000u) : __uint_as_float(tw << 16);
            cfv[r] = wok[k] ? (wdiag[k] ? a : a + b) : 0.0f;
        }
        *reinterpret_cast<f32x4*>(smem + LDS_SPARE + (w + 4 * k) * 1024 + lane * 16) = cfv;
    }

    STAMP(1);
    __builtin_amdgcn_sched_barrier(0);
    asm volatile("s_waitcnt vmcnt(9) lgkmcnt(0)\n\ts_barrier" ::: "memory");
    __builtin_amdgcn_sched_barrier(0);
    STAMP(2);

    f32x4 acc[8][4];
#pragma unroll
    for (int t = 0; t < 8; ++t)
#pragma unroll
        for (int u = 0; u < 4; ++u) acc[t][u] = (f32x4){0.f, 0.f, 0.f, 0.f};

    bf16x8 af[8];
#pragma unroll
    for (int t = 0; t < 8; ++t) af[t] = lds_read128(smem, addr[t][0]);
    const unsigned long long mynonce = *reinterpret_cast<const unsigned long long*>(smem + LDS_NONCE);
    const int prow0 = mt * 128 + kh * 64 + w * 16;
    unsigned praw[4][7], paddr[4][7];
    u32x4 bc[4], bn[4];
#pragma unroll
    for (int p = 0; p < 4; ++p) {
        const u32x2 a = braw[0][2 * p], b = braw[0][2 * p + 1];
        bc[0][p] = __builtin_amdgcn_perm(b[0], a[0], 0x05040100u);
        bc[1][p] = __builtin_amdgcn_perm(b[0], a[0], 0x07060302u);
        bc[2][p] = __builtin_amdgcn_perm(b[1], a[1], 0x05040100u);
        bc[3][p] = __builtin_amdgcn_perm(b[1], a[1], 0x07060302u);
    }
#pragma unroll
    for (int jj = 0; jj < 8; ++jj)
        braw[0][jj] = *reinterpret_cast<const u32x2*>(w1lane + 3 * 4096 + jj * 512);
    const int pk = lane & 3;
    const int peer = bid ^ (pk == 0 ? 1 : 2 * pk);
    GAS unsigned long long* preq = (GAS unsigned long long*)(ws + WS_REQ) + peer * 8;
    GAS unsigned long long* phel = (GAS unsigned long long*)(ws + WS_HEL) + peer * 8 + pk;
    GAS unsigned long long* mhel = (GAS unsigned long long*)(ws + WS_HEL) + bid * 8 + pk;
    unsigned long long pn = 0, hh = 0;
    __builtin_amdgcn_sched_barrier(0);

#pragma unroll
    for (int s = 0; s < 25; ++s) {
#pragma unroll
        for (int t = 0; t < 8; ++t) {
#pragma unroll
            for (int u = 0; u < 4; ++u)
                acc[t][u] = __builtin_amdgcn_mfma_f32_16x16x32_bf16(af[t], __builtin_bit_cast(bf16x8, bc[u]), acc[t][u], 0, 0, 0);
            if (s + 1 < 24)
                af[t] = lds_read128(smem, addr[t][(s + 1) >> 3] ^ (unsigned)(((s + 1) & 7) << 4));
            else if (s + 1 == 24)
                af[t] = lds_read128(smem, addr[t][3]);
            if (s + 1 < 25) {
                const int p = t >> 1;
                const u32x2 a = braw[(s + 1) % 3][2 * p], b = braw[(s + 1) % 3][2 * p + 1];
                if ((t & 1) == 0) {
                    bn[0][p] = __builtin_amdgcn_perm(b[0], a[0], 0x05040100u);
                    bn[1][p] = __builtin_amdgcn_perm(b[0], a[0], 0x07060302u);
                } else {
                    bn[2][p] = __builtin_amdgcn_perm(b[1], a[1], 0x05040100u);
                    bn[3][p] = __builtin_amdgcn_perm(b[1], a[1], 0x07060302u);
                    if (s + 4 < 24) {
                        braw[(s + 1) % 3][2 * p] = *reinterpret_cast<const u32x2*>(w1lane + (s + 4) * 4096 + (2 * p) * 512);
                        braw[(s + 1) % 3][2 * p + 1] = *reinterpret_cast<const u32x2*>(w1lane + (s + 4) * 4096 + (2 * p + 1) * 512);
                    } else if (s + 4 == 24) {
                        braw[(s + 1) % 3][2 * p] = *reinterpret_cast<const u32x2*>(w1last + (2 * p) * 512);
                        braw[(s + 1) % 3][2 * p + 1] = *reinterpret_cast<const u32x2*>(w1last + (2 * p + 1) * 512);
                    }
                }
            }
            if (s >= 2 && s < 6 && t < 7) {
                const int fld = (t < 6) ? 16 * t + c : (c < 4 ? 96 + c : 0);
                praw[s - 2][t] = (unsigned)idx[(prow0 + nt * 4 + (s - 2)) * NF + fld];
            }
            if (s >= 12 && s < 16 && t < 7) {
                unsigned v = praw[s - 12][t];
                if (t == 6) v = (c < 4) ? v : 1000u;
                unsigned a = v * 128u + (((unsigned)q ^ ((v >> 1) & 7u)) << 4);
                asm volatile("" : "+v"(a));
                paddr[s - 12][t] = a;
            }
            if (t == 7) {
                if (s == 8) {
                    unsigned d0 = __hip_atomic_load((GAS unsigned*)(ws + WS_X) + ((bid ^ pk) * 4 + w) * 1024, RLX_AGENT);
                    asm volatile("" :: "v"(d0));
                }
                if (s == 0) pn = __hip_atomic_load(preq, RLX_AGENT);
                if (s == 6) __hip_atomic_store(phel, pn, RLX_AGENT);
                if (s == 16) hh = __hip_atomic_load(mhel, RLX_AGENT);
                if (s == 20) { asm volatile("" : "+v"(preq) : "v"(hh)); pn = __hip_atomic_load(preq, RLX_AGENT); }
                if (s == 24) __hip_atomic_store(phel, pn, RLX_AGENT);
            }
            __builtin_amdgcn_sched_group_barrier(0x008, 1, 0);
            __builtin_amdgcn_sched_group_barrier(0x002, 1, 0);
            __builtin_amdgcn_sched_group_barrier(0x020, 1, 0);
            __builtin_amdgcn_sched_group_barrier(0x008, 1, 0);
            __builtin_amdgcn_sched_group_barrier(0x002, 1, 0);
            __builtin_amdgcn_sched_group_barrier(0x020, 1, 0);
            __builtin_amdgcn_sched_group_barrier(0x008, 1, 0);
            __builtin_amdgcn_sched_group_barrier(0x002, 2, 0);
            __builtin_amdgcn_sched_group_barrier(0x008, 1, 0);
            __builtin_amdgcn_sched_group_barrier(0x100, 1, 0);
            __builtin_amdgcn_sched_group_barrier(0x002, 1, 0);
        }
        if (s + 1 < 25) {
#pragma unroll
            for (int u = 0; u < 4; ++u) { asm volatile("" : "+v"(bn[u])); bc[u] = bn[u]; }
        }
        if (s == 3) asm volatile("" :: "v"(warm));
        __builtin_amdgcn_sched_barrier(0);
    }
    asm volatile("" ::: "memory");
    if (!__all(hh == mynonce)) {
        unsigned spins = 0;
        for (;;) {
            pn = __hip_atomic_load(preq, RLX_AGENT);
            __hip_atomic_store(phel, pn, RLX_AGENT);
            hh = __hip_atomic_load(mhel, RLX_AGENT);
            if (__all(hh == mynonce)) break;
            __builtin_amdgcn_s_sleep(8);
            if (++spins > (1u << 22)) break;
        }
        asm volatile("" : "+v"(preq) : "v"(hh));
        pn = __hip_atomic_load(preq, RLX_AGENT);
        __hip_atomic_store(phel, pn, RLX_AGENT);
    }

    STAMP(3);
    f32x4 cf[28];
#pragma unroll
    for (int t = 0; t < 28; ++t)
        cf[t] = *reinterpret_cast<const f32x4*>(smem + LDS_SPARE + t * 1024 + lane * 16);
#pragma unroll
    for (int t = 0; t < 28; ++t) asm volatile("" : "+v"(cf[t]));
    __syncthreads();
#pragma unroll
    for (int r = 1; r < 4; ++r) {
        char* dst = smem + LDS_SPARE + (((w + r) & 3) * 8192) + lane * 16;
#pragma unroll
        for (int h = 0; h < 2; ++h)
#pragma unroll
            for (int u = 0; u < 4; ++u)
                *reinterpret_cast<f32x4*>(dst + (h * 4 + u) * 1024) = acc[r * 2 + h][u];
        __syncthreads();
        const char* src = smem + LDS_SPARE + w * 8192 + lane * 16;
#pragma unroll
        for (int h = 0; h < 2; ++h)
#pragma unroll
            for (int u = 0; u < 4; ++u)
                acc[h][u] += *reinterpret_cast<const f32x4*>(src + (h * 4 + u) * 1024);
        if (r < 3) __syncthreads();
    }

    STAMP(4);
    const __amdgpu_buffer_rsrc_t xr = __builtin_amdgcn_make_buffer_rsrc(ws + WS_X, 0, 1024 * 4096, 0x00020000);
    {
        const unsigned off = (unsigned)((bid * 4 + w) * 4096 + lane * 16);
#pragma unroll
        for (int u = 0; u < 4; ++u)
            __builtin_amdgcn_raw_buffer_store_b128(__builtin_bit_cast(u32x4, acc[1][u]), xr, off + u * 1024, 0, 16);
    }

    STAMP(5);
    STAMP(6);
    float lp[4];
    GAS unsigned long long* gr = (GAS unsigned long long*)(ws + WS_GRAN) + ((mt * 2 + kh) * 4 + w) * 128;
    bf16x8 e[2][7][2];
#pragma unroll
    for (int I = 0; I < 7; ++I) {
        e[0][I][0] = lds_read128(smem, paddr[0][I]);
        e[0][I][1] = lds_read128(smem, paddr[0][I] ^ 64u);
    }
#pragma unroll
    for (int i = 0; i < 4; ++i) {
#pragma unroll
        for (int I = 0; I < 7; ++I) { asm volatile("" : "+a"(e[i & 1][I][0])); asm volatile("" : "+a"(e[i & 1][I][1])); }
        if (i < 3) {
#pragma unroll
            for (int I = 0; I < 7; ++I) {
                e[(i + 1) & 1][I][0] = lds_read128(smem, paddr[i + 1][I]);
                e[(i + 1) & 1][I][1] = lds_read128(smem, paddr[i + 1][I] ^ 64u);
            }
        }
        if (i == 2) {
            asm volatile("s_waitcnt vmcnt(0)" ::: "memory");
            __syncthreads();
            if (tid == 0)
                __hip_atomic_store((GAS unsigned long long*)(ws + WS_TAG) + (bid ^ 1) * 8, pn, RLX_AGENT);
        }
        if (i == 3) {
            GAS unsigned long long* fl = (GAS unsigned long long*)(ws + WS_TAG) + bid * 8;
            unsigned spins = 0;
            while (!__all(__hip_atomic_load(fl, RLX_AGENT) == mynonce)) {
                __builtin_amdgcn_s_sleep(4);
                if (++spins > (1u << 22)) break;
            }
            __builtin_amdgcn_fence(__ATOMIC_ACQUIRE, "wavefront");
            const unsigned off = (unsigned)(((bid ^ 1) * 4 + w) * 4096 + lane * 16);
#pragma unroll
            for (int u = 0; u < 4; ++u)
                acc[0][u] += __builtin_bit_cast(f32x4, __builtin_amdgcn_raw_buffer_load_b128(xr, off + u * 1024, 0, 16));
            const float bb[4] = {__uint_as_float(b1p[0] << 16), __uint_as_float(b1p[0] & 0xffff0000u),
                                 __uint_as_float(b1p[1] << 16), __uint_as_float(b1p[1] & 0xffff0000u)};
            const float ww[4] = {__uint_as_float(w2p[0] << 16), __uint_as_float(w2p[0] & 0xffff0000u),
                                 __uint_as_float(w2p[1] << 16), __uint_as_float(w2p[1] & 0xffff0000u)};
            float rs[4];
#pragma unroll
            for (int r = 0; r < 4; ++r) {
                float a = 0.f;
#pragma unroll
                for (int u = 0; u < 4; ++u) {
                    float y = bfround(acc[0][u][r]);
                    y = bfround(y + bb[u]);
                    const float hh = bfround(gelu_tanh(y));
                    a = fmaf(hh, ww[u], a);
                }
                rs[r] = a;
            }
#pragma unroll
            for (int m = 1; m < 16; m <<= 1)
#pragma unroll
                for (int r = 0; r < 4; ++r) rs[r] += __shfl_xor(rs[r], m, 64);
            const int xq = nt ^ q;
            unsigned tlo = (unsigned)__shfl((int)(unsigned)pn, xq, 64), thi = (unsigned)__shfl((int)(unsigned)(pn >> 32), xq, 64);
            if (xq == 0) { tlo = (unsigned)mynonce; thi = (unsigned)(mynonce >> 32); }
            if (c == 0) {
#pragma unroll
                for (int r = 0; r < 4; ++r) {
                    const unsigned long long vb = (unsigned long long)__float_as_uint(rs[r]);
                    __hip_atomic_store(gr + (nt * 16 + q * 4 + r) * 2, ((unsigned long long)tlo << 32) | vb, RLX_AGENT);
                    __hip_atomic_store(gr + (nt * 16 + q * 4 + r) * 2 + 1, ((unsigned long long)thi << 32) | vb, RLX_AGENT);
                }
            }
        }
        __builtin_amdgcn_sched_barrier(0);
        if (i == 0) RSTAMP(9);
        if (i == 1) RSTAMP(13);
        if (i == 2) RSTAMP(14);
        if (i == 3) RSTAMP(15);
        float s0 = 0.f, s1 = 0.f, s2 = 0.f, s3 = 0.f;
        f32x4 d[3];
        int t = 0;
#pragma unroll
        for (int I = 0; I < 7; ++I) {
#pragma unroll
            for (int J = I; J < 7; ++J, ++t) {
                if (t < 2) {
                    asm volatile(
                        "s_nop 1\n\t"
                        "v_mfma_f32_16x16x32_bf16 %0, %1, %2, 0\n\t"
                        "v_mfma_f32_16x16x32_bf16 %0, %3, %4, %0"
                        : "=&v"(d[t % 3])
                        : "a"(e[i & 1][I][0]), "a"(e[i & 1][J][0]), "a"(e[i & 1][I][1]), "a"(e[i & 1][J][1]));
                } else {
                    const f32x4 dp = d[(t + 1) % 3];
                    const f32x4 cp = cf[t - 2];
                    if (t == 2) {
                        asm volatile(
                            "v_mfma_f32_16x16x32_bf16 %0, %5, %6, 0\n\t"
                            "s_nop 4\n\t"
                            "v_fmac_f32 %1, %9, %13\n\t"
                            "v_fmac_f32 %2, %10, %14\n\t"
                            "v_mfma_f32_16x16x32_bf16 %0, %7, %8, %0\n\t"
                            "v_fmac_f32 %3, %11, %15\n\t"
                            "v_fmac_f32 %4, %12, %16"
                            : "=&v"(d[t % 3]), "+v"(s0), "+v"(s1), "+v"(s2), "+v"(s3)
                            : "a"(e[i & 1][I][0]), "a"(e[i & 1][J][0]), "a"(e[i & 1][I][1]), "a"(e[i & 1][J][1]),
                              "v"(cp[0]), "v"(cp[1]), "v"(cp[2]), "v"(cp[3]),
                              "v"(dp[0]), "v"(dp[1]), "v"(dp[2]), "v"(dp[3]));
                    } else {
                        asm volatile(
                            "v_mfma_f32_16x16x32_bf16 %0, %5, %6, 0\n\t"
                            "v_fmac_f32 %1, %9, %13\n\t"
                            "v_fmac_f32 %2, %10, %14\n\t"
                            "v_mfma_f32_16x16x32_bf16 %0, %7, %8, %0\n\t"
                            "v_fmac_f32 %3, %11, %15\n\t"
                            "v_fmac_f32 %4, %12, %16"
                            : "=&v"(d[t % 3]), "+v"(s0), "+v"(s1), "+v"(s2), "+v"(s3)
                            : "a"(e[i & 1][I][0]), "a"(e[i & 1][J][0]), "a"(e[i & 1][I][1]), "a"(e[i & 1][J][1]),
                              "v"(cp[0]), "v"(cp[1]), "v"(cp[2]), "v"(cp[3]),
                              "v"(dp[0]), "v"(dp[1]), "v"(dp[2]), "v"(dp[3]));
                    }
                }
            }
        }
        {
            const f32x4 da = d[26 % 3], db = d[27 % 3], ca = cf[26], cb = cf[27];
            asm volatile(
                "s_nop 1\n\t"
                "v_fmac_f32 %0, %4, %8\n\t"
                "v_fmac_f32 %1, %5, %9\n\t"
                "v_fmac_f32 %2, %6, %10\n\t"
                "v_fmac_f32 %3, %7, %11\n\t"
                "v_fmac_f32 %0, %12, %16\n\t"
                "v_fmac_f32 %1, %13, %17\n\t"
                "v_fmac_f32 %2, %14, %18\n\t"
                "v_fmac_f32 %3, %15, %19"
                : "+v"(s0), "+v"(s1), "+v"(s2), "+v"(s3)
                : "v"(ca[0]), "v"(ca[1]), "v"(ca[2]), "v"(ca[3]), "v"(da[0]), "v"(da[1]), "v"(da[2]), "v"(da[3]),
                  "v"(cb[0]), "v"(cb[1]), "v"(cb[2]), "v"(cb[3]), "v"(db[0]), "v"(db[1]), "v"(db[2]), "v"(db[3]));
        }
        lp[i] = (s0 + s1) + (s2 + s3);
        __builtin_amdgcn_sched_barrier(0);
    }
#pragma unroll
    for (int m = 1; m < 64; m <<= 1)
#pragma unroll
        for (int i = 0; i < 4; ++i) lp[i] += __shfl_xor(lp[i], m, 64);

    {
        float v = 0.f;
        unsigned spins = 0;
        for (;;) {
            bool ok = true;
            if (lane < 16) {
                const unsigned long long xa = __hip_atomic_load(gr + ((lane >> 2) * 16 + nt * 4 + (lane & 3)) * 2, RLX_AGENT);
                const unsigned long long xb = __hip_atomic_load(gr + ((lane >> 2) * 16 + nt * 4 + (lane & 3)) * 2 + 1, RLX_AGENT);
                v = __uint_as_float((unsigned)xa);
                ok = (xa >> 32) == (mynonce & 0xffffffffull) && (xb >> 32) == (mynonce >> 32) && (unsigned)xa == (unsigned)xb;
            }
            if (__all(ok)) break;
            __builtin_amdgcn_s_sleep(8);
            if (++spins > (1u << 22)) break;
        }
        STAMP(7);
        RSTAMP(11);
        v += __shfl_xor(v, 4, 64);
        v += __shfl_xor(v, 8, 64);
        const float lpv = (lane & 2) ? ((lane & 1) ? lp[3] : lp[2]) : ((lane & 1) ? lp[1] : lp[0]);
        if (lane < 4) out[prow0 + nt * 4 + lane] = __float2bfloat16(v + b2p + lpv);
    }
    STAMP(8);
    RSTAMP(12);
}

extern "C" void kernel_launch(void* const* d_in, const int* in_sizes, int n_in,
                              void* d_out, int out_size, void* d_ws, size_t ws_size,
                              hipStream_t stream) {
    const int* idx = (const int*)d_in[0];
    const unsigned short* table = (const unsigned short*)d_in[1];
    const unsigned short* wp = (const unsigned short*)d_in[2];
    const unsigned short* w1 = (const unsigned short*)d_in[3];
    const unsigned short* b1 = (const unsigned short*)d_in[4];
    const unsigned short* w2 = (const unsigned short*)d_in[5];
    const unsigned short* b2 = (const unsigned short*)d_in[6];
    dcp_kernel<<<256, 256, 0, stream>>>(idx, table, wp, w1, b1, w2, b2, (__hip_bfloat16*)d_out, (char*)d_ws);
}
```

```cpp
#include <hip/hip_runtime.h>
#include <hip/hip_bf16.h>
#include <stdint.h>

typedef __attribute__((ext_vector_type(8))) short bf16x8;
typedef __attribute__((ext_vector_type(4))) float f32x4;
typedef __attribute__((ext_vector_type(4))) unsigned u32x4;
typedef __attribute__((ext_vector_type(2))) unsigned u32x2;
typedef __attribute__((ext_vector_type(4), aligned(4))) int i32x4u;
typedef __attribute__((ext_vector_type(2), aligned(4))) int i32x2u;
typedef __attribute__((ext_vector_type(16))) float f32x16;

#define GAS __attribute__((address_space(1)))
#define LAS __attribute__((address_space(3)))
#define RLX_AGENT __ATOMIC_RELAXED, __HIP_MEMORY_SCOPE_AGENT

constexpr int NF = 100;
constexpr int LDS_ZROW = 128000;
constexpr int LDS_SPARE = 128128;
constexpr int LDS_TOTAL = LDS_SPARE + 32768 + 16;

constexpr size_t WS_REQ = 0;
constexpr size_t WS_HEL = 16384;
constexpr size_t WS_TAG = 32768;
constexpr size_t WS_GRAN = 65536;
constexpr size_t WS_X = 65536 + 524288;
constexpr int LDS_NONCE = LDS_SPARE + 32768;

#define RSTAMP(n) do {} while (0)
#define STAMP(n) do {} while (0)

__device__ __forceinline__ float bf2f(unsigned short u) { return __uint_as_float(((unsigned)u) << 16); }
__device__ __forceinline__ float bfround(float x) {
    return __bfloat162float(__float2bfloat16(x));
}
__device__ __forceinline__ float gelu_tanh(float x) {
    const float u = 0.7978845608028654f * (x + 0.044715f * x * x * x);
    const float e = __expf(2.0f * u);
    const float t = 1.0f - 2.0f * __builtin_amdgcn_rcpf(e + 1.0f);
    return 0.5f * x * (1.0f + t);
}
__device__ __forceinline__ bf16x8 lds_read128(const char* smem, unsigned off) {
    return *reinterpret_cast<const bf16x8*>(smem + off);
}

#define MLP_STEP \
_Pragma("unroll") \
        for (int t = 0; t < 4; ++t) { \
_Pragma("unroll") \
            for (int u = 0; u < 2; ++u) \
                acc[t >> 1][t & 1][u] = __builtin_amdgcn_mfma_f32_32x32x16_bf16(af[t], __builtin_bit_cast(bf16x8, bc[u]), acc[t >> 1][t & 1][u], 0, 0, 0); \
            if (s + 1 < 48) \
                af[t] = lds_read128(smem, addr[t][(s + 1) >> 3] ^ (unsigned)(((s + 1) & 7) << 4)); \
            else if (s + 1 < 50) \
                af[t] = lds_read128(smem, addr[t][6] ^ (unsigned)((s + 1 - 48) << 4)); \
            if (s + 1 < 50) { \
                const unsigned a = braw[(s + 1) & 3][2 * t], b = braw[(s + 1) & 3][2 * t + 1]; \
                bn[0][t] = __builtin_amdgcn_perm(b, a, sel0); \
                bn[1][t] = __builtin_amdgcn_perm(b, a, sel1); \
                if (s + 5 < 48) { \
                    braw[(s + 1) & 3][2 * t] = *reinterpret_cast<const unsigned*>(w1lane + (s + 5) * 4096 + (2 * t) * 512); \
                    braw[(s + 1) & 3][2 * t + 1] = *reinterpret_cast<const unsigned*>(w1lane + (s + 5) * 4096 + (2 * t + 1) * 512); \
                } else if (s + 5 < 50) { \
                    braw[(s + 1) & 3][2 * t] = *reinterpret_cast<const unsigned*>(w1last + (s + 5 - 48) * 4096 + (2 * t) * 512); \
                    braw[(s + 1) & 3][2 * t + 1] = *reinterpret_cast<const unsigned*>(w1last + (s + 5 - 48) * 4096 + (2 * t + 1) * 512); \
                } \
            } \
            if (s >= 4 && s < 11) { \
                const int I = s - 4; \
                const int fld = (I < 6) ? 16 * I + c : (c < 4 ? 96 + c : 0); \
                praw[t][I] = (unsigned)idx[(prow0 + t) * NF + fld]; \
            } \
            if (s >= 24 && s < 31) { \
                const int I = s - 24; \
                unsigned v = praw[t][I]; \
                if (I == 6) v = (c < 4) ? v : 1000u; \
                unsigned a = v * 128u + (((unsigned)q ^ ((v >> 1) & 7u)) << 4); \
                asm volatile("" : "+v"(a)); \
                paddr[t][I] = a; \
            } \
            if (t == 3) { \
                if (s == 16) {    \
                    unsigned d0 = __hip_atomic_load((GAS unsigned*)(ws + WS_X) + ((bid ^ pk) * 4 + w) * 1024, RLX_AGENT); \
                    asm volatile("" :: "v"(d0)); \
                } \
                if (s == 0) pn = __hip_atomic_load(preq, RLX_AGENT); \
                if (s == 12) __hip_atomic_store(phel, pn, RLX_AGENT); \
                if (s == 32) hh = __hip_atomic_load(mhel, RLX_AGENT); \
                if (s == 40) { asm volatile("" : "+v"(preq) : "v"(hh)); pn = __hip_atomic_load(preq, RLX_AGENT); } \
                if (s == 49) __hip_atomic_store(phel, pn, RLX_AGENT); \
            } \
            __builtin_amdgcn_sched_group_barrier(0x008, 1, 0);    \
            __builtin_amdgcn_sched_group_barrier(0x002, 2, 0);    \
            __builtin_amdgcn_sched_group_barrier(0x020, 2, 0);    \
            __builtin_amdgcn_sched_group_barrier(0x008, 1, 0);    \
            __builtin_amdgcn_sched_group_barrier(0x100, 1, 0);    \
            __builtin_amdgcn_sched_group_barrier(0x002, 3, 0);    \
            __builtin_amdgcn_sched_group_barrier(0x020, 1, 0);    \
        } \
        if (s + 1 < 50) { \
_Pragma("unroll") \
            for (int u = 0; u < 2; ++u) { asm volatile("" : "+v"(bn[u])); bc[u] = bn[u]; } \
        } \
        if (s == 6) asm volatile("" :: "v"(warm)); \
        __builtin_amdgcn_sched_barrier(0);

__global__ __launch_bounds__(256, 1)
void dcp_kernel(const int* __restrict__ idx, const unsigned short* __restrict__ table,
                const unsigned short* __restrict__ wp, const unsigned short* __restrict__ w1,
                const unsigned short* __restrict__ b1, const unsigned short* __restrict__ w2,
                const unsigned short* __restrict__ b2, __hip_bfloat16* __restrict__ out,
                char* __restrict__ ws)
{
    __shared__ __attribute__((aligned(16))) char smem[LDS_TOTAL];

    const int tid = threadIdx.x;
    const int lane = tid & 63;
    const int w = __builtin_amdgcn_readfirstlane(tid >> 6);
    const int bid = blockIdx.x;
    const int mt = bid >> 3, c8 = bid & 7, nt = c8 >> 1, kh = bid & 1;
    const int c = lane & 15, q = lane >> 4;

    RSTAMP(10);
    STAMP(0);
    if (tid == 0) {
        const unsigned long long n0 = __builtin_amdgcn_s_memrealtime();
        __hip_atomic_store((GAS unsigned long long*)(ws + WS_REQ) + bid * 8, n0, RLX_AGENT);
        *reinterpret_cast<unsigned long long*>(smem + LDS_NONCE) = n0;
    }
    const int kw = kh * 4 + w;
    const int c32 = lane & 31, hl = lane >> 5;
    const int U = kw * 2 + hl;
    i32x4u iva[4]; i32x2u ivb[4]; int ivc[4];
#pragma unroll
    for (int hh = 0; hh < 2; ++hh) {
#pragma unroll
        for (int jr = 0; jr < 2; ++jr) {
            const int sample = mt * 128 + (hh ^ kh) * 64 + ((w >> 1) ^ jr) * 32 + c32;
            const int* ip = idx + sample * NF;
            iva[hh * 2 + jr] = *reinterpret_cast<const i32x4u*>(ip + 6 * U);
            ivb[hh * 2 + jr] = *reinterpret_cast<const i32x2u*>(ip + 6 * U + 4);
            ivc[hh * 2 + jr] = ip[96 + (U >> 2)];
        }
    }
    const float b1v = bf2f(b1[nt * 64 + 2 * c32 + (w & 1)]);
    const float w2v = bf2f(w2[nt * 64 + 2 * c32 + (w & 1)]);
    const float b2v = bf2f(b2[0]);
    unsigned short wa[7][4]; u32x2 wt[7]; bool wok[7], wdiag[7];
#pragma unroll
    for (int k = 0; k < 7; ++k) {
        const int t = w + 4 * k;
        const unsigned long long Ilo = 0x2221111110000000ull, Ihi = 0x0000655444333322ull;
        const unsigned long long Jlo = 0x4326543216543210ull, Jhi = 0x0000665654654365ull;
        const int I = (int)(((t < 16 ? Ilo : Ihi) >> ((t & 15) * 4)) & 15);
        const int J = (int)(((t < 16 ? Jlo : Jhi) >> ((t & 15) * 4)) & 15);
        const int jcol = 16 * J + c;
        const int jc = jcol < NF ? jcol : NF - 1;
        const int i0 = 16 * I + 4 * q;
        const int i0c = i0 < NF ? i0 : NF - 4;
        wok[k] = (i0 < NF) && (jcol < NF);
        wdiag[k] = (I == J);
        wt[k] = *reinterpret_cast<const u32x2*>(wp + jc * NF + i0c);
#pragma unroll
        for (int r = 0; r < 4; ++r) wa[k][r] = wp[(i0c + r) * NF + jc];
    }
    const char* w1lane = (const char*)w1 + (size_t)(384 * U) * 512 + (nt * 64 + 2 * c32) * 2;
    const char* w1last = (const char*)w1 + (size_t)((96 + (U >> 2)) * 64 + ((2 * U) & 7) * 8) * 512 + (nt * 64 + 2 * c32) * 2;
    unsigned braw[4][8];
#pragma unroll
    for (int s = 0; s < 3; ++s)
#pragma unroll
        for (int jj = 0; jj < 8; ++jj)
            braw[s][jj] = *reinterpret_cast<const unsigned*>(w1lane + s * 4096 + jj * 512);
    __builtin_amdgcn_sched_barrier(0);
    {
        const int srow = lane >> 3, slot = lane & 7;
        const unsigned lds0 = (unsigned)(uintptr_t)(LAS char*)smem;
#pragma unroll
        for (int k = 0; k < 32; ++k) {
            int p = w + 4 * k; p = p < 125 ? p : 124;
            const int row = p * 8 + srow;
            const int chunk = slot ^ ((row >> 1) & 7);
            const char* src = (const char*)table + row * 128 + chunk * 16;
            unsigned keep;
            asm volatile("s_mov_b32 %0, m0\n\ts_mov_b32 m0, %2\n\ts_nop 0\n\tglobal_load_lds_dwordx4 %1, off\n\ts_mov_b32 m0, %0"
                         : "=&s"(keep) : "v"(src), "s"(lds0 + (unsigned)p * 1024u) : "memory");
        }
        if (tid < 8) *reinterpret_cast<u32x4*>(smem + LDS_ZROW + tid * 16) = (u32x4){0u, 0u, 0u, 0u};
    }
    __builtin_amdgcn_sched_barrier(0);
#pragma unroll
    for (int jj = 0; jj < 8; ++jj)
        braw[3][jj] = *reinterpret_cast<const unsigned*>(w1lane + 3 * 4096 + jj * 512);
    unsigned warm;
    {
        const int id = mt * 4 + w;
        int row = (id < 48) ? kh * 3072 + id * 64 + lane : 6144 + kh * 128 + (id & 1) * 64 + lane;
        warm = *reinterpret_cast<const unsigned*>((const char*)w1 + (size_t)row * 512 + nt * 128);
    }
    __builtin_amdgcn_sched_barrier(0);

    float b1p = b1v, w2p = w2v, b2p = b2v;
    asm volatile("" : "+v"(b1p), "+v"(w2p), "+v"(b2p));
    unsigned addr[4][7];
#pragma unroll
    for (int t = 0; t < 4; ++t) {
#pragma unroll
        for (int fi = 0; fi < 7; ++fi) {
            const unsigned v = (unsigned)(fi < 4 ? iva[t][fi] : (fi < 6 ? ivb[t][fi - 4] : ivc[t]));
            unsigned a = v * 128u + (((v >> 1) & 7u) << 4);
            if (fi == 6) a ^= (unsigned)((2 * U) & 7) << 4;
            asm volatile("" : "+v"(a));
            addr[t][fi] = a;
        }
    }
#pragma unroll
    for (int k = 0; k < 7; ++k) {
        f32x4 cfv;
#pragma unroll
        for (int r = 0; r < 4; ++r) {
            const float a = bf2f(wa[k][r]);
            const unsigned tw = (r & 2) ? wt[k][1] : wt[k][0];
            const float b = (r & 1) ? __uint_as_float(tw & 0xffff0000u) : __uint_as_float(tw << 16);
            cfv[r] = wok[k] ? (wdiag[k] ? a : a + b) : 0.0f;
        }
        *reinterpret_cast<f32x4*>(smem + LDS_SPARE + (w + 4 * k) * 1024 + lane * 16) = cfv;
    }

    STAMP(1);
    __builtin_amdgcn_sched_barrier(0);
    asm volatile("s_waitcnt vmcnt(9) lgkmcnt(0)\n\ts_barrier" ::: "memory");
    __builtin_amdgcn_sched_barrier(0);
    STAMP(2);

    f32x16 acc[2][2][2];
#pragma unroll
    for (int a0 = 0; a0 < 2; ++a0)
#pragma unroll
        for (int a1 = 0; a1 < 2; ++a1)
#pragma unroll
            for (int a2 = 0; a2 < 2; ++a2)
#pragma unroll
                for (int r = 0; r < 16; ++r) acc[a0][a1][a2][r] = 0.f;

    bf16x8 af[4];
#pragma unroll
    for (int t = 0; t < 4; ++t) af[t] = lds_read128(smem, addr[t][0]);
    const unsigned long long mynonce = *reinterpret_cast<const unsigned long long*>(smem + LDS_NONCE);
    const int fm = nt * 2 + (w & 1);
    const int prow0 = mt * 128 + kh * 64 + (w >> 1) * 32 + fm * 4;
    unsigned praw[4][7], paddr[4][7];
    const unsigned sel0 = (w & 1) ? 0x07060302u : 0x05040100u, sel1 = (w & 1) ? 0x05040100u : 0x07060302u;
    u32x4 bc[2], bn[2];
#pragma unroll
    for (int p = 0; p < 4; ++p) {
        bc[0][p] = __builtin_amdgcn_perm(braw[0][2 * p + 1], braw[0][2 * p], sel0);
        bc[1][p] = __builtin_amdgcn_perm(braw[0][2 * p + 1], braw[0][2 * p], sel1);
    }
#pragma unroll
    for (int jj = 0; jj < 8; ++jj)
        braw[0][jj] = *reinterpret_cast<const unsigned*>(w1lane + 4 * 4096 + jj * 512);
    const int pk = lane & 3;
    const int peer = bid ^ (pk == 0 ? 1 : 2 * pk);
    GAS unsigned long long* preq = (GAS unsigned long long*)(ws + WS_REQ) + peer * 8;
    GAS unsigned long long* phel = (GAS unsigned long long*)(ws + WS_HEL) + peer * 8 + pk;
    GAS unsigned long long* mhel = (GAS unsigned long long*)(ws + WS_HEL) + bid * 8 + pk;
    unsigned long long pn = 0, hh = 0;
    __builtin_amdgcn_sched_barrier(0);

#pragma unroll
    for (int s = 0; s < 17; ++s) { MLP_STEP }
#pragma unroll
    for (int s = 17; s < 34; ++s) { MLP_STEP }
#pragma unroll
    for (int s = 34; s < 50; ++s) { MLP_STEP }
    asm volatile("" ::: "memory");
    if (!__all(hh == mynonce)) {
        unsigned spins = 0;
        for (;;) {
            pn = __hip_atomic_load(preq, RLX_AGENT);
            __hip_atomic_store(phel, pn, RLX_AGENT);
            hh = __hip_atomic_load(mhel, RLX_AGENT);
            if (__all(hh == mynonce)) break;
            __builtin_amdgcn_s_sleep(8);
            if (++spins > (1u << 22)) break;
        }
        asm volatile("" : "+v"(preq) : "v"(hh));
        pn = __hip_atomic_load(preq, RLX_AGENT);
        __hip_atomic_store(phel, pn, RLX_AGENT);
    }

    STAMP(3);
    f32x4 cf[28];
#pragma unroll
    for (int t = 0; t < 28; ++t)
        cf[t] = *reinterpret_cast<const f32x4*>(smem + LDS_SPARE + t * 1024 + lane * 16);
#pragma unroll
    for (int t = 0; t < 28; ++t) asm volatile("" : "+v"(cf[t]));
    __syncthreads();
    f32x4 own[4], snd[4];
#pragma unroll
    for (int u = 0; u < 4; ++u)
#pragma unroll
        for (int r = 0; r < 4; ++r) { own[u][r] = acc[0][0][0][u * 4 + r]; snd[u][r] = acc[1][0][0][u * 4 + r]; }
#pragma unroll
    for (int r = 1; r < 4; ++r) {
        char* dst = smem + LDS_SPARE + ((w ^ r) * 8192) + lane * 16;
#pragma unroll
        for (int h = 0; h < 2; ++h)
#pragma unroll
            for (int u = 0; u < 4; ++u) {
                f32x4 v;
#pragma unroll
                for (int k = 0; k < 4; ++k) v[k] = acc[h][r >> 1][r & 1][u * 4 + k];
                *reinterpret_cast<f32x4*>(dst + (h * 4 + u) * 1024) = v;
            }
        __syncthreads();
        const char* src = smem + LDS_SPARE + w * 8192 + lane * 16;
#pragma unroll
        for (int u = 0; u < 4; ++u) {
            own[u] += *reinterpret_cast<const f32x4*>(src + u * 1024);
            snd[u] += *reinterpret_cast<const f32x4*>(src + (4 + u) * 1024);
        }
        if (r < 3) __syncthreads();
    }

    STAMP(4);
    const __amdgpu_buffer_rsrc_t xr = __builtin_amdgcn_make_buffer_rsrc(ws + WS_X, 0, 1024 * 4096, 0x00020000);
    {
        const unsigned off = (unsigned)((bid * 4 + w) * 4096 + lane * 16);
#pragma unroll
        for (int u = 0; u < 4; ++u)
            __builtin_amdgcn_raw_buffer_store_b128(__builtin_bit_cast(u32x4, snd[u]), xr, off + u * 1024, 0, 16);
    }

    STAMP(5);
    STAMP(6);
    float lp[4];
    GAS unsigned long long* gr = (GAS unsigned long long*)(ws + WS_GRAN) + ((mt * 2 + kh) * 2 + (w >> 1)) * 512;
    bf16x8 e[2][7][2];
#pragma unroll
    for (int I = 0; I < 7; ++I) {
        e[0][I][0] = lds_read128(smem, paddr[0][I]);
        e[0][I][1] = lds_read128(smem, paddr[0][I] ^ 64u);
    }
#pragma unroll
    for (int i = 0; i < 4; ++i) {
#pragma unroll
        for (int I = 0; I < 7; ++I) { asm volatile("" : "+a"(e[i & 1][I][0])); asm volatile("" : "+a"(e[i & 1][I][1])); }
        if (i < 3) {
#pragma unroll
            for (int I = 0; I < 7; ++I) {
                e[(i + 1) & 1][I][0] = lds_read128(smem, paddr[i + 1][I]);
                e[(i + 1) & 1][I][1] = lds_read128(smem, paddr[i + 1][I] ^ 64u);
            }
        }
        if (i == 2) {
            asm volatile("s_waitcnt vmcnt(0)" ::: "memory");
            __syncthreads();
            if (tid == 0)
                __hip_atomic_store((GAS unsigned long long*)(ws + WS_TAG) + (bid ^ 1) * 8, pn, RLX_AGENT);
        }
        if (i == 3) {
            GAS unsigned long long* fl = (GAS unsigned long long*)(ws + WS_TAG) + bid * 8;
            unsigned spins = 0;
            while (!__all(__hip_atomic_load(fl, RLX_AGENT) == mynonce)) {
                __builtin_amdgcn_s_sleep(4);
                if (++spins > (1u << 22)) break;
            }
            __builtin_amdgcn_fence(__ATOMIC_ACQUIRE, "wavefront");
            const unsigned off = (unsigned)(((bid ^ 1) * 4 + w) * 4096 + lane * 16);
#pragma unroll
            for (int u = 0; u < 4; ++u)
                own[u] += __builtin_bit_cast(f32x4, __builtin_amdgcn_raw_buffer_load_b128(xr, off + u * 1024, 0, 16));
            float g[16];
#pragma unroll
            for (int r = 0; r < 16; ++r) {
                float y = bfround(own[r >> 2][r & 3]);
                y = bfround(y + b1p);
                g[r] = bfround(gelu_tanh(y)) * w2p;
            }
#pragma unroll
            for (int st = 0; st < 4; ++st) {
                const int half = 8 >> st, m = 16 >> st;
                const bool up = (lane & m) != 0;
#pragma unroll
                for (int k = 0; k < half; ++k) {
                    const float keep = up ? g[k + half] : g[k];
                    const float give = up ? g[k] : g[k + half];
                    g[k] = keep + __shfl_xor(give, m, 64);
                }
            }
            g[0] += __shfl_xor(g[0], 1, 64);
            const int reg = (lane >> 1) & 15;
            const int row = (reg & 3) + 8 * (reg >> 2) + 4 * hl;
            const int xq = nt ^ (row >> 3);
            unsigned tlo = (unsigned)__shfl((int)(unsigned)pn, xq, 64), thi = (unsigned)__shfl((int)(unsigned)(pn >> 32), xq, 64);
            if (xq == 0) { tlo = (unsigned)mynonce; thi = (unsigned)(mynonce >> 32); }
            if ((lane & 1) == 0) {
                const unsigned long long vb = (unsigned long long)__float_as_uint(g[0]);
                __hip_atomic_store(gr + (fm * 32 + row) * 2, ((unsigned long long)tlo << 32) | vb, RLX_AGENT);
                __hip_atomic_store(gr + (fm * 32 + row) * 2 + 1, ((unsigned long long)thi << 32) | vb, RLX_AGENT);
            }
        }
        __builtin_amdgcn_sched_barrier(0);
        if (i == 0) RSTAMP(9);
        if (i == 1) RSTAMP(13);
        if (i == 2) RSTAMP(14);
        if (i == 3) RSTAMP(15);
        float s0 = 0.f, s1 = 0.f, s2 = 0.f, s3 = 0.f;
        f32x4 d[3];
        int t = 0;
#pragma unroll
        for (int I = 0; I < 7; ++I) {
#pragma unroll
            for (int J = I; J < 7; ++J, ++t) {
                if (t < 2) {
                    asm volatile(
                        "s_nop 1\n\t"
                        "v_mfma_f32_16x16x32_bf16 %0, %1, %2, 0\n\t"
                        "v_mfma_f32_16x16x32_bf16 %0, %3, %4, %0"
                        : "=&v"(d[t % 3])
                        : "a"(e[i & 1][I][0]), "a"(e[i & 1][J][0]), "a"(e[i & 1][I][1]), "a"(e[i & 1][J][1]));
                } else {
                    const f32x4 dp = d[(t + 1) % 3];
                    const f32x4 cp = cf[t - 2];
                    if (t == 2) {
                        asm volatile(
                            "v_mfma_f32_16x16x32_bf16 %0, %5, %6, 0\n\t"
                            "s_nop 4\n\t"
                            "v_fmac_f32 %1, %9, %13\n\t"
                            "v_fmac_f32 %2, %10, %14\n\t"
                            "v_mfma_f32_16x16x32_bf16 %0, %7, %8, %0\n\t"
                            "v_fmac_f32 %3, %11, %15\n\t"
                            "v_fmac_f32 %4, %12, %16"
                            : "=&v"(d[t % 3]), "+v"(s0), "+v"(s1), "+v"(s2), "+v"(s3)
                            : "a"(e[i & 1][I][0]), "a"(e[i & 1][J][0]), "a"(e[i & 1][I][1]), "a"(e[i & 1][J][1]),
                              "v"(cp[0]), "v"(cp[1]), "v"(cp[2]), "v"(cp[3]),
                              "v"(dp[0]), "v"(dp[1]), "v"(dp[2]), "v"(dp[3]));
                    } else {
                        asm volatile(
                            "v_mfma_f32_16x16x32_bf16 %0, %5, %6, 0\n\t"
                            "v_fmac_f32 %1, %9, %13\n\t"
                            "v_fmac_f32 %2, %10, %14\n\t"
                            "v_mfma_f32_16x16x32_bf16 %0, %7, %8, %0\n\t"
                            "v_fmac_f32 %3, %11, %15\n\t"
                            "v_fmac_f32 %4, %12, %16"
                            : "=&v"(d[t % 3]), "+v"(s0), "+v"(s1), "+v"(s2), "+v"(s3)
                            : "a"(e[i & 1][I][0]), "a"(e[i & 1][J][0]), "a"(e[i & 1][I][1]), "a"(e[i & 1][J][1]),
                              "v"(cp[0]), "v"(cp[1]), "v"(cp[2]), "v"(cp[3]),
                              "v"(dp[0]), "v"(dp[1]), "v"(dp[2]), "v"(dp[3]));
                    }
                }
            }
        }
        {
            const f32x4 da = d[26 % 3], db = d[27 % 3], ca = cf[26], cb = cf[27];
            asm volatile(
                "s_nop 1\n\t"
                "v_fmac_f32 %0, %4, %8\n\t"
                "v_fmac_f32 %1, %5, %9\n\t"
                "v_fmac_f32 %2, %6, %10\n\t"
                "v_fmac_f32 %3, %7, %11\n\t"
                "v_fmac_f32 %0, %12, %16\n\t"
                "v_fmac_f32 %1, %13, %17\n\t"
                "v_fmac_f32 %2, %14, %18\n\t"
                "v_fmac_f32 %3, %15, %19"
                : "+v"(s0), "+v"(s1), "+v"(s2), "+v"(s3)
                : "v"(ca[0]), "v"(ca[1]), "v"(ca[2]), "v"(ca[3]), "v"(da[0]), "v"(da[1]), "v"(da[2]), "v"(da[3]),
                  "v"(cb[0]), "v"(cb[1]), "v"(cb[2]), "v"(cb[3]), "v"(db[0]), "v"(db[1]), "v"(db[2]), "v"(db[3]));
        }
        lp[i] = (s0 + s1) + (s2 + s3);
        __builtin_amdgcn_sched_barrier(0);
    }
#pragma unroll
    for (int m = 1; m < 64; m <<= 1)
#pragma unroll
        for (int i = 0; i < 4; ++i) lp[i] += __shfl_xor(lp[i], m, 64);

    {
        float v = 0.f;
        unsigned spins = 0;
        for (;;) {
            bool ok = true;
            if (lane < 32) {
                const unsigned long long xa = __hip_atomic_load(gr + ((lane >> 2) * 32 + fm * 4 + (lane & 3)) * 2, RLX_AGENT);
                const unsigned long long xb = __hip_atomic_load(gr + ((lane >> 2) * 32 + fm * 4 + (lane & 3)) * 2 + 1, RLX_AGENT);
                v = __uint_as_float((unsigned)xa);
                ok = (xa >> 32) == (mynonce & 0xffffffffull) && (xb >> 32) == (mynonce >> 32) && (unsigned)xa == (unsigned)xb;
            }
            if (__all(ok)) break;
            __builtin_amdgcn_s_sleep(8);
            if (++spins > (1u << 22)) break;
        }
        STAMP(7);
        RSTAMP(11);
        v += __shfl_xor(v, 4, 64);
        v += __shfl_xor(v, 8, 64);
        v += __shfl_xor(v, 16, 64);
        const float lpv = (lane & 2) ? ((lane & 1) ? lp[3] : lp[2]) : ((lane & 1) ? lp[1] : lp[0]);
        if (lane < 4) out[prow0 + lane] = __float2bfloat16(v + b2p + lpv);
    }
    STAMP(8);
    RSTAMP(12);
}

extern "C" void kernel_launch(void* const* d_in, const int* in_sizes, int n_in,
                              void* d_out, int out_size, void* d_ws, size_t ws_size,
                              hipStream_t stream) {
    const int* idx = (const int*)d_in[0];
    const unsigned short* table = (const unsigned short*)d_in[1];
    const unsigned short* wp = (const unsigned short*)d_in[2];
    const unsigned short* w1 = (const unsigned short*)d_in[3];
    const unsigned short* b1 = (const unsigned short*)d_in[4];
    const unsigned short* w2 = (const unsigned short*)d_in[5];
    const unsigned short* b2 = (const unsigned short*)d_in[6];
    dcp_kernel<<<256, 256, 0, stream>>>(idx, table, wp, w1, b1, w2, b2, (__hip_bfloat16*)d_out, (char*)d_ws);
}
```

```cpp
#include <hip/hip_runtime.h>
#include <hip/hip_bf16.h>
#include <stdint.h>

typedef __attribute__((ext_vector_type(8))) short bf16x8;
typedef __attribute__((ext_vector_type(4))) float f32x4;
typedef __attribute__((ext_vector_type(4))) unsigned u32x4;
typedef __attribute__((ext_vector_type(2))) unsigned u32x2;
typedef __attribute__((ext_vector_type(3), aligned(4))) int i32x3u;

#define GAS __attribute__((address_space(1)))
#define LAS __attribute__((address_space(3)))
#define RLX_AGENT __ATOMIC_RELAXED, __HIP_MEMORY_SCOPE_AGENT

constexpr int NF = 100;
constexpr int LDS_ZROW = 128000;
constexpr int LDS_SPARE = 128128;
constexpr int LDS_TOTAL = LDS_SPARE + 32768 + 16;

constexpr size_t WS_REQ = 0;
constexpr size_t WS_HEL = 16384;
constexpr size_t WS_TAG = 32768;
constexpr size_t WS_GRAN = 65536;
constexpr size_t WS_X = 65536 + 262144;
constexpr int LDS_NONCE = LDS_SPARE + 32768;

#define RSTAMP(n) do {} while (0)
#define STAMP(n) do {} while (0)

__device__ __forceinline__ float bf2f(unsigned short u) { return __uint_as_float(((unsigned)u) << 16); }
__device__ __forceinline__ float bfround(float x) {
    return __bfloat162float(__float2bfloat16(x));
}
__device__ __forceinline__ float gelu_tanh(float x) {
    const float u = 0.7978845608028654f * (x + 0.044715f * x * x * x);
    const float e = __expf(2.0f * u);
    const float t = 1.0f - 2.0f * __builtin_amdgcn_rcpf(e + 1.0f);
    return 0.5f * x * (1.0f + t);
}
__device__ __forceinline__ bf16x8 lds_read128(const char* smem, unsigned off) {
    return *reinterpret_cast<const bf16x8*>(smem + off);
}

__global__ __launch_bounds__(256, 1)
void dcp_kernel(const int* __restrict__ idx, const unsigned short* __restrict__ table,
                const unsigned short* __restrict__ wp, const unsigned short* __restrict__ w1,
                const unsigned short* __restrict__ b1, const unsigned short* __restrict__ w2,
                const unsigned short* __restrict__ b2, __hip_bfloat16* __restrict__ out,
                char* __restrict__ ws)
{
    __shared__ __attribute__((aligned(16))) char smem[LDS_TOTAL];

    const int tid = threadIdx.x;
    const int lane = tid & 63;
    const int w = __builtin_amdgcn_readfirstlane(tid >> 6);
    const int bid = blockIdx.x;
    const int mt = bid >> 3, nt = ((bid >> 1) ^ mt) & 3, kh = bid & 1;
    const int c = lane & 15, q = lane >> 4;

    RSTAMP(10);
    STAMP(0);
    if (tid == 0) {
        const unsigned long long n0 = __builtin_amdgcn_s_memrealtime();
        __hip_atomic_store((GAS unsigned long long*)(ws + WS_REQ) + bid * 8, n0, RLX_AGENT);
        *reinterpret_cast<unsigned long long*>(smem + LDS_NONCE) = n0;
    }
    const int kw = kh * 4 + w;
    const int U = kw * 4 + q;
    i32x3u iv[8]; int iv3[8];
#pragma unroll
    for (int j = 0; j < 4; ++j) {
#pragma unroll
        for (int h = 0; h < 2; ++h) {
            const int o = (w + j) & 3;
            const int sample = mt * 128 + (h ^ kh) * 64 + o * 16 + c;
            const int* ip = idx + sample * NF;
            iv[j * 2 + h] = *reinterpret_cast<const i32x3u*>(ip + 3 * U);
            iv3[j * 2 + h] = ip[96 + (U >> 3)];
        }
    }
    const u32x2 b1v = *reinterpret_cast<const u32x2*>(b1 + nt * 64 + 4 * c);
    const u32x2 w2v = *reinterpret_cast<const u32x2*>(w2 + nt * 64 + 4 * c);
    const float b2v = bf2f(b2[0]);
    unsigned short wa[7][4]; u32x2 wt[7]; bool wok[7], wdiag[7];
#pragma unroll
    for (int k = 0; k < 7; ++k) {
        const int t = w + 4 * k;
        const unsigned long long Ilo = 0x2221111110000000ull, Ihi = 0x0000655444333322ull;
        const unsigned long long Jlo = 0x4326543216543210ull, Jhi = 0x0000665654654365ull;
        const int I = (int)(((t < 16 ? Ilo : Ihi) >> ((t & 15) * 4)) & 15);
        const int J = (int)(((t < 16 ? Jlo : Jhi) >> ((t & 15) * 4)) & 15);
        const int jcol = 16 * J + c;
        const int jc = jcol < NF ? jcol : NF - 1;
        const int i0 = 16 * I + 4 * q;
        const int i0c = i0 < NF ? i0 : NF - 4;
        wok[k] = (i0 < NF) && (jcol < NF);
        wdiag[k] = (I == J);
        wt[k] = *reinterpret_cast<const u32x2*>(wp + jc * NF + i0c);
#pragma unroll
        for (int r = 0; r < 4; ++r) wa[k][r] = wp[(i0c + r) * NF + jc];
    }
    const char* w1lane = (const char*)w1 + (size_t)(192 * U) * 512 + (nt * 64 + 4 * c) * 2;
    const char* w1last = (const char*)w1 + (size_t)((96 + (U >> 3)) * 64 + (U & 7) * 8) * 512 + (nt * 64 + 4 * c) * 2;
    u32x2 braw[3][8];
#pragma unroll
    for (int s = 0; s < 2; ++s)
#pragma unroll
        for (int jj = 0; jj < 8; ++jj)
            braw[s][jj] = *reinterpret_cast<const u32x2*>(w1lane + s * 4096 + jj * 512);
    __builtin_amdgcn_sched_barrier(0);
    {
        const int srow = lane >> 3, slot = lane & 7;
        const unsigned lds0 = (unsigned)(uintptr_t)(LAS char*)smem;
#pragma unroll
        for (int k = 0; k < 32; ++k) {
            int p = w + 4 * k; p = p < 125 ? p : 124;
            const int row = p * 8 + srow;
            const int chunk = slot ^ ((row >> 1) & 7);
            const char* src = (const char*)table + row * 128 + chunk * 16;
            unsigned keep;
            asm volatile("s_mov_b32 %0, m0\n\ts_mov_b32 m0, %2\n\ts_nop 0\n\tglobal_load_lds_dwordx4 %1, off\n\ts_mov_b32 m0, %0"
                         : "=&s"(keep) : "v"(src), "s"(lds0 + (unsigned)p * 1024u) : "memory");
        }
        if (tid < 8) *reinterpret_cast<u32x4*>(smem + LDS_ZROW + tid * 16) = (u32x4){0u, 0u, 0u, 0u};
    }
    __builtin_amdgcn_sched_barrier(0);
#pragma unroll
    for (int jj = 0; jj < 8; ++jj)
        braw[2][jj] = *reinterpret_cast<const u32x2*>(w1lane + 2 * 4096 + jj * 512);
    unsigned warm;
    {
        const int L0 = (mt * 4 + w) * 100 + lane, L1 = L0 + (lane < 36 ? 64 : 0);
        const int r0 = L0 >> 2, r1 = L1 >> 2;
        const int row0 = r0 < 3072 ? kh * 3072 + r0 : 6144 + kh * 128 + (r0 - 3072);
        const int row1 = r1 < 3072 ? kh * 3072 + r1 : 6144 + kh * 128 + (r1 - 3072);
        warm = *reinterpret_cast<const unsigned*>((const char*)w1 + (size_t)row0 * 512 + (L0 & 3) * 128) ^
               *reinterpret_cast<const unsigned*>((const char*)w1 + (size_t)row1 * 512 + (L1 & 3) * 128);
    }
    __builtin_amdgcn_sched_barrier(0);

    u32x2 b1p = b1v, w2p = w2v; float b2p = b2v;
    asm volatile("" : "+v"(b1p), "+v"(w2p), "+v"(b2p));
    unsigned addr[8][4];
#pragma unroll
    for (int t = 0; t < 8; ++t) {
#pragma unroll
        for (int fi = 0; fi < 4; ++fi) {
            const unsigned v = (unsigned)(fi < 3 ? iv[t][fi] : iv3[t]);
            unsigned a = v * 128u + (((v >> 1) & 7u) << 4);
            if (fi == 3) a ^= (unsigned)(U & 7) << 4;
            asm volatile("" : "+v"(a));
            addr[t][fi] = a;
        }
    }
#pragma unroll
    for (int k = 0; k < 7; ++k) {
        f32x4 cfv;
#pragma unroll
        for (int r = 0; r < 4; ++r) {
            const float a = bf2f(wa[k][r]);
            const unsigned tw = (r & 2) ? wt[k][1] : wt[k][0];
            const float b = (r & 1) ? __uint_as_float(tw & 0xffff0000u) : __uint_as_float(tw << 16);
            cfv[r] = wok[k] ? (wdiag[k] ? a : a + b) : 0.0f;
        }
        *reinterpret_cast<f32x4*>(smem + LDS_SPARE + (w + 4 * k) * 1024 + lane * 16) = cfv;
    }

    STAMP(1);
    __builtin_amdgcn_sched_barrier(0);
    asm volatile("s_waitcnt vmcnt(10) lgkmcnt(0)\n\ts_barrier" ::: "memory");
    __builtin_amdgcn_sched_barrier(0);
    STAMP(2);

    f32x4 acc[8][4];
#pragma unroll
    for (int t = 0; t < 8; ++t)
#pragma unroll
        for (int u = 0; u < 4; ++u) acc[t][u] = (f32x4){0.f, 0.f, 0.f, 0.f};

    bf16x8 af[8];
#pragma unroll
    for (int t = 0; t < 8; ++t) af[t] = lds_read128(smem, addr[t][0]);
    const unsigned long long mynonce = *reinterpret_cast<const unsigned long long*>(smem + LDS_NONCE);
    const int prow0 = mt * 128 + kh * 64 + w * 16;
    unsigned praw[4][7], paddr[4][7];
    u32x4 bc[4], bn[4];
#pragma unroll
    for (int p = 0; p < 4; ++p) {
        const u32x2 a = braw[0][2 * p], b = braw[0][2 * p + 1];
        bc[0][p] = __builtin_amdgcn_perm(b[0], a[0], 0x05040100u);
        bc[1][p] = __builtin_amdgcn_perm(b[0], a[0], 0x07060302u);
        bc[2][p] = __builtin_amdgcn_perm(b[1], a[1], 0x05040100u);
        bc[3][p] = __builtin_amdgcn_perm(b[1], a[1], 0x07060302u);
    }
#pragma unroll
    for (int jj = 0; jj < 8; ++jj)
        braw[0][jj] = *reinterpret_cast<const u32x2*>(w1lane + 3 * 4096 + jj * 512);
    const int pk = lane & 3;
    const int peer = bid ^ (pk == 0 ? 1 : 2 * pk);
    GAS unsigned long long* preq = (GAS unsigned long long*)(ws + WS_REQ) + peer * 8;
    GAS unsigned long long* phel = (GAS unsigned long long*)(ws + WS_HEL) + peer * 8 + pk;
    GAS unsigned long long* mhel = (GAS unsigned long long*)(ws + WS_HEL) + bid * 8 + pk;
    unsigned long long pn = 0, hh = 0;
    __builtin_amdgcn_sched_barrier(0);

#pragma unroll
    for (int s = 0; s < 25; ++s) {
#pragma unroll
        for (int t = 0; t < 8; ++t) {
#pragma unroll
            for (int u = 0; u < 4; ++u)
                acc[t][u] = __builtin_amdgcn_mfma_f32_16x16x32_bf16(af[t], __builtin_bit_cast(bf16x8, bc[u]), acc[t][u], 0, 0, 0);
            if (s + 1 < 24)
                af[t] = lds_read128(smem, addr[t][(s + 1) >> 3] ^ (unsigned)(((s + 1) & 7) << 4));
            else if (s + 1 == 24)
                af[t] = lds_read128(smem, addr[t][3]);
            if (s + 1 < 25) {
                const int p = t >> 1;
                const u32x2 a = braw[(s + 1) % 3][2 * p], b = braw[(s + 1) % 3][2 * p + 1];
                if ((t & 1) == 0) {
                    bn[0][p] = __builtin_amdgcn_perm(b[0], a[0], 0x05040100u);
                    bn[1][p] = __builtin_amdgcn_perm(b[0], a[0], 0x07060302u);
                } else {
                    bn[2][p] = __builtin_amdgcn_perm(b[1], a[1], 0x05040100u);
                    bn[3][p] = __builtin_amdgcn_perm(b[1], a[1], 0x07060302u);
                    if (s + 4 < 24) {
                        braw[(s + 1) % 3][2 * p] = *reinterpret_cast<const u32x2*>(w1lane + (s + 4) * 4096 + (2 * p) * 512);
                        braw[(s + 1) % 3][2 * p + 1] = *reinterpret_cast<const u32x2*>(w1lane + (s + 4) * 4096 + (2 * p + 1) * 512);
                    } else if (s + 4 == 24) {
                        braw[(s + 1) % 3][2 * p] = *reinterpret_cast<const u32x2*>(w1last + (2 * p) * 512);
                        braw[(s + 1) % 3][2 * p + 1] = *reinterpret_cast<const u32x2*>(w1last + (2 * p + 1) * 512);
                    }
                }
            }
            if (s >= 2 && s < 6 && t < 7) {
                const int fld = (t < 6) ? 16 * t + c : (c < 4 ? 96 + c : 0);
                praw[s - 2][t] = (unsigned)idx[(prow0 + nt * 4 + (s - 2)) * NF + fld];
            }
            if (s >= 12 && s < 16 && t < 7) {
                unsigned v = praw[s - 12][t];
                if (t == 6) v = (c < 4) ? v : 1000u;
                unsigned a = v * 128u + (((unsigned)q ^ ((v >> 1) & 7u)) << 4);
                asm volatile("" : "+v"(a));
                paddr[s - 12][t] = a;
            }
            if (t == 7) {
                if (s == 8) {
                    unsigned d0 = __hip_atomic_load((GAS unsigned*)(ws + WS_X) + ((bid ^ pk) * 4 + w) * 1024, RLX_AGENT);
                    asm volatile("" :: "v"(d0));
                }
                if (s == 0) pn = __hip_atomic_load(preq, RLX_AGENT);
                if (s == 6) __hip_atomic_store(phel, pn, RLX_AGENT);
                if (s == 16) hh = __hip_atomic_load(mhel, RLX_AGENT);
                if (s == 20) { asm volatile("" : "+v"(preq) : "v"(hh)); pn = __hip_atomic_load(preq, RLX_AGENT); }
                if (s == 24) __hip_atomic_store(phel, pn, RLX_AGENT);
            }
            __builtin_amdgcn_sched_group_barrier(0x008, 1, 0);
            __builtin_amdgcn_sched_group_barrier(0x002, 1, 0);
            __builtin_amdgcn_sched_group_barrier(0x020, 1, 0);
            __builtin_amdgcn_sched_group_barrier(0x008, 1, 0);
            __builtin_amdgcn_sched_group_barrier(0x002, 1, 0);
            __builtin_amdgcn_sched_group_barrier(0x020, 1, 0);
            __builtin_amdgcn_sched_group_barrier(0x008, 1, 0);
            __builtin_amdgcn_sched_group_barrier(0x002, 2, 0);
            __builtin_amdgcn_sched_group_barrier(0x008, 1, 0);
            __builtin_amdgcn_sched_group_barrier(0x100, 1, 0);
            __builtin_amdgcn_sched_group_barrier(0x002, 1, 0);
        }
        if (s + 1 < 25) {
#pragma unroll
            for (int u = 0; u < 4; ++u) { asm volatile("" : "+v"(bn[u])); bc[u] = bn[u]; }
        }
        if (s == 3) asm volatile("" :: "v"(warm));
        __builtin_amdgcn_sched_barrier(0);
    }
    asm volatile("" ::: "memory");
    if (!__all(hh == mynonce)) {
        unsigned spins = 0;
        for (;;) {
            pn = __hip_atomic_load(preq, RLX_AGENT);
            __hip_atomic_store(phel, pn, RLX_AGENT);
            hh = __hip_atomic_load(mhel, RLX_AGENT);
            if (__all(hh == mynonce)) break;
            __builtin_amdgcn_s_sleep(8);
            if (++spins > (1u << 22)) break;
        }
        asm volatile("" : "+v"(preq) : "v"(hh));
        pn = __hip_atomic_load(preq, RLX_AGENT);
        __hip_atomic_store(phel, pn, RLX_AGENT);
    }

    STAMP(3);
    f32x4 cf[28];
#pragma unroll
    for (int t = 0; t < 28; ++t)
        cf[t] = *reinterpret_cast<const f32x4*>(smem + LDS_SPARE + t * 1024 + lane * 16);
#pragma unroll
    for (int t = 0; t < 28; ++t) asm volatile("" : "+v"(cf[t]));
    __syncthreads();
#pragma unroll
    for (int r = 1; r < 4; ++r) {
        char* dst = smem + LDS_SPARE + (((w + r) & 3) * 8192) + lane * 16;
#pragma unroll
        for (int h = 0; h < 2; ++h)
#pragma unroll
            for (int u = 0; u < 4; ++u)
                *reinterpret_cast<f32x4*>(dst + (h * 4 + u) * 1024) = acc[r * 2 + h][u];
        __syncthreads();
        const char* src = smem + LDS_SPARE + w * 8192 + lane * 16;
#pragma unroll
        for (int h = 0; h < 2; ++h)
#pragma unroll
            for (int u = 0; u < 4; ++u)
                acc[h][u] += *reinterpret_cast<const f32x4*>(src + (h * 4 + u) * 1024);
        if (r < 3) __syncthreads();
    }

    STAMP(4);
    const __amdgpu_buffer_rsrc_t xr = __builtin_amdgcn_make_buffer_rsrc(ws + WS_X, 0, 1024 * 4096, 0x00020000);
    {
        const unsigned off = (unsigned)((bid * 4 + w) * 4096 + lane * 16);
#pragma unroll
        for (int u = 0; u < 4; ++u)
            __builtin_amdgcn_raw_buffer_store_b128(__builtin_bit_cast(u32x4, acc[1][u]), xr, off + u * 1024, 0, 16);
    }

    STAMP(5);
    STAMP(6);
    float lp[4];
    f32x4 rx[4];
    GAS unsigned long long* gr = (GAS unsigned long long*)(ws + WS_GRAN) + ((mt * 2 + kh) * 4 + w) * 128;
    bf16x8 e[2][7][2];
#pragma unroll
    for (int I = 0; I < 7; ++I) {
        e[0][I][0] = lds_read128(smem, paddr[0][I]);
        e[0][I][1] = lds_read128(smem, paddr[0][I] ^ 64u);
    }
#pragma unroll
    for (int i = 0; i < 4; ++i) {
#pragma unroll
        for (int I = 0; I < 7; ++I) { asm volatile("" : "+a"(e[i & 1][I][0])); asm volatile("" : "+a"(e[i & 1][I][1])); }
        if (i < 3) {
#pragma unroll
            for (int I = 0; I < 7; ++I) {
                e[(i + 1) & 1][I][0] = lds_read128(smem, paddr[i + 1][I]);
                e[(i + 1) & 1][I][1] = lds_read128(smem, paddr[i + 1][I] ^ 64u);
            }
        }
        if (i == 1) {
            asm volatile("s_waitcnt vmcnt(0)" ::: "memory");
            __syncthreads();
            if (tid == 0)
                __hip_atomic_store((GAS unsigned long long*)(ws + WS_TAG) + (bid ^ 1) * 8, pn, RLX_AGENT);
        }
        if (i == 2) {
            GAS unsigned long long* fl = (GAS unsigned long long*)(ws + WS_TAG) + bid * 8;
            unsigned spins = 0;
            while (!__all(__hip_atomic_load(fl, RLX_AGENT) == mynonce)) {
                __builtin_amdgcn_s_sleep(2);
                if (++spins > (1u << 22)) break;
            }
            __builtin_amdgcn_fence(__ATOMIC_ACQUIRE, "wavefront");
            const unsigned off = (unsigned)(((bid ^ 1) * 4 + w) * 4096 + lane * 16);
#pragma unroll
            for (int u = 0; u < 4; ++u)
                rx[u] = __builtin_bit_cast(f32x4, __builtin_amdgcn_raw_buffer_load_b128(xr, off + u * 1024, 0, 16));
        }
        if (i == 3) {
#pragma unroll
            for (int u = 0; u < 4; ++u) acc[0][u] += rx[u];
            const float bb[4] = {__uint_as_float(b1p[0] << 16), __uint_as_float(b1p[0] & 0xffff0000u),
                                 __uint_as_float(b1p[1] << 16), __uint_as_float(b1p[1] & 0xffff0000u)};
            const float ww[4] = {__uint_as_float(w2p[0] << 16), __uint_as_float(w2p[0] & 0xffff0000u),
                                 __uint_as_float(w2p[1] << 16), __uint_as_float(w2p[1] & 0xffff0000u)};
            float rs[4];
#pragma unroll
            for (int r = 0; r < 4; ++r) {
                float a = 0.f;
#pragma unroll
                for (int u = 0; u < 4; ++u) {
                    float y = bfround(acc[0][u][r]);
                    y = bfround(y + bb[u]);
                    const float hh = bfround(gelu_tanh(y));
                    a = fmaf(hh, ww[u], a);
                }
                rs[r] = a;
            }
#pragma unroll
            for (int m = 1; m < 16; m <<= 1)
#pragma unroll
                for (int r = 0; r < 4; ++r) rs[r] += __shfl_xor(rs[r], m, 64);
            const int xq = nt ^ q;
            unsigned tlo = (unsigned)__shfl((int)(unsigned)pn, xq, 64), thi = (unsigned)__shfl((int)(unsigned)(pn >> 32), xq, 64);
            if (xq == 0) { tlo = (unsigned)mynonce; thi = (unsigned)(mynonce >> 32); }
            if (c == 0) {
#pragma unroll
                for (int r = 0; r < 4; ++r) {
                    const unsigned long long vb = (unsigned long long)__float_as_uint(rs[r]);
                    __hip_atomic_store(gr + (nt * 16 + q * 4 + r) * 2, ((unsigned long long)tlo << 32) | vb, RLX_AGENT);
                    __hip_atomic_store(gr + (nt * 16 + q * 4 + r) * 2 + 1, ((unsigned long long)thi << 32) | vb, RLX_AGENT);
                }
            }
        }
        __builtin_amdgcn_sched_barrier(0);
        if (i == 0) RSTAMP(9);
        if (i == 1) RSTAMP(13);
        if (i == 2) RSTAMP(14);
        if (i == 3) RSTAMP(15);
        float s0 = 0.f, s1 = 0.f, s2 = 0.f, s3 = 0.f;
        f32x4 d[3];
        int t = 0;
#pragma unroll
        for (int I = 0; I < 7; ++I) {
#pragma unroll
            for (int J = I; J < 7; ++J, ++t) {
                if (t < 2) {
                    asm volatile(
                        "s_nop 1\n\t"
                        "v_mfma_f32_16x16x32_bf16 %0, %1, %2, 0\n\t"
                        "v_mfma_f32_16x16x32_bf16 %0, %3, %4, %0"
                        : "=&v"(d[t % 3])
                        : "a"(e[i & 1][I][0]), "a"(e[i & 1][J][0]), "a"(e[i & 1][I][1]), "a"(e[i & 1][J][1]));
                } else {
                    const f32x4 dp = d[(t + 1) % 3];
                    const f32x4 cp = cf[t - 2];
                    if (t == 2) {
                        asm volatile(
                            "v_mfma_f32_16x16x32_bf16 %0, %5, %6, 0\n\t"
                            "s_nop 4\n\t"
                            "v_fmac_f32 %1, %9, %13\n\t"
                            "v_fmac_f32 %2, %10, %14\n\t"
                            "v_mfma_f32_16x16x32_bf16 %0, %7, %8, %0\n\t"
                            "v_fmac_f32 %3, %11, %15\n\t"
                            "v_fmac_f32 %4, %12, %16"
                            : "=&v"(d[t % 3]), "+v"(s0), "+v"(s1), "+v"(s2), "+v"(s3)
                            : "a"(e[i & 1][I][0]), "a"(e[i & 1][J][0]), "a"(e[i & 1][I][1]), "a"(e[i & 1][J][1]),
                              "v"(cp[0]), "v"(cp[1]), "v"(cp[2]), "v"(cp[3]),
                              "v"(dp[0]), "v"(dp[1]), "v"(dp[2]), "v"(dp[3]));
                    } else {
                        asm volatile(
                            "v_mfma_f32_16x16x32_bf16 %0, %5, %6, 0\n\t"
                            "v_fmac_f32 %1, %9, %13\n\t"
                            "v_fmac_f32 %2, %10, %14\n\t"
                            "v_mfma_f32_16x16x32_bf16 %0, %7, %8, %0\n\t"
                            "v_fmac_f32 %3, %11, %15\n\t"
                            "v_fmac_f32 %4, %12, %16"
                            : "=&v"(d[t % 3]), "+v"(s0), "+v"(s1), "+v"(s2), "+v"(s3)
                            : "a"(e[i & 1][I][0]), "a"(e[i & 1][J][0]), "a"(e[i & 1][I][1]), "a"(e[i & 1][J][1]),
                              "v"(cp[0]), "v"(cp[1]), "v"(cp[2]), "v"(cp[3]),
                              "v"(dp[0]), "v"(dp[1]), "v"(dp[2]), "v"(dp[3]));
                    }
                }
            }
        }
        {
            const f32x4 da = d[26 % 3], db = d[27 % 3], ca = cf[26], cb = cf[27];
            asm volatile(
                "s_nop 1\n\t"
                "v_fmac_f32 %0, %4, %8\n\t"
                "v_fmac_f32 %1, %5, %9\n\t"
                "v_fmac_f32 %2, %6, %10\n\t"
                "v_fmac_f32 %3, %7, %11\n\t"
                "v_fmac_f32 %0, %12, %16\n\t"
                "v_fmac_f32 %1, %13, %17\n\t"
                "v_fmac_f32 %2, %14, %18\n\t"
                "v_fmac_f32 %3, %15, %19"
                : "+v"(s0), "+v"(s1), "+v"(s2), "+v"(s3)
                : "v"(ca[0]), "v"(ca[1]), "v"(ca[2]), "v"(ca[3]), "v"(da[0]), "v"(da[1]), "v"(da[2]), "v"(da[3]),
                  "v"(cb[0]), "v"(cb[1]), "v"(cb[2]), "v"(cb[3]), "v"(db[0]), "v"(db[1]), "v"(db[2]), "v"(db[3]));
        }
        lp[i] = (s0 + s1) + (s2 + s3);
        __builtin_amdgcn_sched_barrier(0);
    }
#pragma unroll
    for (int m = 1; m < 64; m <<= 1)
#pragma unroll
        for (int i = 0; i < 4; ++i) lp[i] += __shfl_xor(lp[i], m, 64);

    {
        float v = 0.f;
        unsigned spins = 0;
        for (;;) {
            bool ok = true;
            if (lane < 16) {
                const unsigned long long xa = __hip_atomic_load(gr + ((lane >> 2) * 16 + nt * 4 + (lane & 3)) * 2, RLX_AGENT);
                const unsigned long long xb = __hip_atomic_load(gr + ((lane >> 2) * 16 + nt * 4 + (lane & 3)) * 2 + 1, RLX_AGENT);
                v = __uint_as_float((unsigned)xa);
                ok = (xa >> 32) == (mynonce & 0xffffffffull) && (xb >> 32) == (mynonce >> 32) && (unsigned)xa == (unsigned)xb;
            }
            if (__all(ok)) break;
            __builtin_amdgcn_s_sleep(8);
            if (++spins > (1u << 22)) break;
        }
        STAMP(7);
        RSTAMP(11);
        v += __shfl_xor(v, 4, 64);
        v += __shfl_xor(v, 8, 64);
        const float lpv = (lane & 2) ? ((lane & 1) ? lp[3] : lp[2]) : ((lane & 1) ? lp[1] : lp[0]);
        if (lane < 4) out[prow0 + nt * 4 + lane] = __float2bfloat16(v + b2p + lpv);
    }
    STAMP(8);
    RSTAMP(12);
}

extern "C" void kernel_launch(void* const* d_in, const int* in_sizes, int n_in,
                              void* d_out, int out_size, void* d_ws, size_t ws_size,
                              hipStream_t stream) {
    const int* idx = (const int*)d_in[0];
    const unsigned short* table = (const unsigned short*)d_in[1];
    const unsigned short* wp = (const unsigned short*)d_in[2];
    const unsigned short* w1 = (const unsigned short*)d_in[3];
    const unsigned short* b1 = (const unsigned short*)d_in[4];
    const unsigned short* w2 = (const unsigned short*)d_in[5];
    const unsigned short* b2 = (const unsigned short*)d_in[6];
    dcp_kernel<<<256, 256, 0, stream>>>(idx, table, wp, w1, b1, w2, b2, (__hip_bfloat16*)d_out, (char*)d_ws);
}
```

```cpp
#include <hip/hip_runtime.h>
#include <hip/hip_bf16.h>
#include <stdint.h>

typedef __attribute__((ext_vector_type(8))) short bf16x8;
typedef __attribute__((ext_vector_type(4))) float f32x4;
typedef __attribute__((ext_vector_type(4))) unsigned u32x4;
typedef __attribute__((ext_vector_type(2))) unsigned u32x2;
typedef __attribute__((ext_vector_type(3), aligned(4))) int i32x3u;

#define GAS __attribute__((address_space(1)))
#define LAS __attribute__((address_space(3)))
#define RLX_AGENT __ATOMIC_RELAXED, __HIP_MEMORY_SCOPE_AGENT

constexpr int NF = 100;
constexpr int LDS_ZROW = 128000;
constexpr int LDS_SPARE = 128128;
constexpr int LDS_TOTAL = LDS_SPARE + 32768 + 16;

constexpr size_t WS_REQ = 0;
constexpr size_t WS_HEL = 16384;
constexpr size_t WS_TAG = 32768;
constexpr size_t WS_GRAN = 65536;
constexpr size_t WS_X = 65536 + 262144;
constexpr int LDS_NONCE = LDS_SPARE + 32768;


__device__ __forceinline__ float bf2f(unsigned short u) { return __uint_as_float(((unsigned)u) << 16); }
__device__ __forceinline__ float bfround(float x) {
    return __bfloat162float(__float2bfloat16(x));
}
__device__ __forceinline__ float gelu_tanh(float x) {
    const float u = 0.7978845608028654f * (x + 0.044715f * x * x * x);
    const float e = __expf(2.0f * u);
    const float t = 1.0f - 2.0f * __builtin_amdgcn_rcpf(e + 1.0f);
    return 0.5f * x * (1.0f + t);
}
__device__ __forceinline__ bf16x8 lds_read128(const char* smem, unsigned off) {
    return *reinterpret_cast<const bf16x8*>(smem + off);
}

__global__ __launch_bounds__(256, 1)
void dcp_kernel(const int* __restrict__ idx, const unsigned short* __restrict__ table,
                const unsigned short* __restrict__ wp, const unsigned short* __restrict__ w1,
                const unsigned short* __restrict__ b1, const unsigned short* __restrict__ w2,
                const unsigned short* __restrict__ b2, __hip_bfloat16* __restrict__ out,
                char* __restrict__ ws)
{
    __shared__ __attribute__((aligned(16))) char smem[LDS_TOTAL];

    const int tid = threadIdx.x;
    const int lane = tid & 63;
    const int w = __builtin_amdgcn_readfirstlane(tid >> 6);
    const int bid = blockIdx.x;
    const int mt = bid >> 3, c8 = bid & 7, nt = c8 >> 1, kh = bid & 1;
    const int c = lane & 15, q = lane >> 4;
    if (tid == 0) {
        const unsigned long long n0 = __builtin_amdgcn_s_memrealtime();
        __hip_atomic_store((GAS unsigned long long*)(ws + WS_REQ) + bid * 8, n0, RLX_AGENT);
        *reinterpret_cast<unsigned long long*>(smem + LDS_NONCE) = n0;
    }
    const int kw = kh * 4 + w;
    const int U = kw * 4 + q;
    i32x3u iv[8]; int iv3[8];
#pragma unroll
    for (int j = 0; j < 4; ++j) {
#pragma unroll
        for (int h = 0; h < 2; ++h) {
            const int o = (w + j) & 3;
            const int sample = mt * 128 + (h ^ kh) * 64 + o * 16 + c;
            const int* ip = idx + sample * NF;
            iv[j * 2 + h] = *reinterpret_cast<const i32x3u*>(ip + 3 * U);
            iv3[j * 2 + h] = ip[96 + (U >> 3)];
        }
    }
    const u32x2 b1v = *reinterpret_cast<const u32x2*>(b1 + nt * 64 + 4 * c);
    const u32x2 w2v = *reinterpret_cast<const u32x2*>(w2 + nt * 64 + 4 * c);
    const float b2v = bf2f(b2[0]);
    unsigned short wa[7][4]; u32x2 wt[7]; bool wok[7], wdiag[7];
#pragma unroll
    for (int k = 0; k < 7; ++k) {
        const int t = w + 4 * k;
        const unsigned long long Ilo = 0x2221111110000000ull, Ihi = 0x0000655444333322ull;
        const unsigned long long Jlo = 0x4326543216543210ull, Jhi = 0x0000665654654365ull;
        const int I = (int)(((t < 16 ? Ilo : Ihi) >> ((t & 15) * 4)) & 15);
        const int J = (int)(((t < 16 ? Jlo : Jhi) >> ((t & 15) * 4)) & 15);
        const int jcol = 16 * J + c;
        const int jc = jcol < NF ? jcol : NF - 1;
        const int i0 = 16 * I + 4 * q;
        const int i0c = i0 < NF ? i0 : NF - 4;
        wok[k] = (i0 < NF) && (jcol < NF);
        wdiag[k] = (I == J);
        wt[k] = *reinterpret_cast<const u32x2*>(wp + jc * NF + i0c);
#pragma unroll
        for (int r = 0; r < 4; ++r) wa[k][r] = wp[(i0c + r) * NF + jc];
    }
    const char* w1lane = (const char*)w1 + (size_t)(192 * U) * 512 + (nt * 64 + 4 * c) * 2;
    const char* w1last = (const char*)w1 + (size_t)((96 + (U >> 3)) * 64 + (U & 7) * 8) * 512 + (nt * 64 + 4 * c) * 2;
    u32x2 braw[3][8];
#pragma unroll
    for (int s = 0; s < 2; ++s)
#pragma unroll
        for (int jj = 0; jj < 8; ++jj)
            braw[s][jj] = *reinterpret_cast<const u32x2*>(w1lane + s * 4096 + jj * 512);
    __builtin_amdgcn_sched_barrier(0);
    {
        const int srow = lane >> 3, slot = lane & 7;
        const unsigned lds0 = (unsigned)(uintptr_t)(LAS char*)smem;
#pragma unroll
        for (int k = 0; k < 32; ++k) {
            int p = w + 4 * k; p = p < 125 ? p : 124;
            const int row = p * 8 + srow;
            const int chunk = slot ^ ((row >> 1) & 7);
            const char* src = (const char*)table + row * 128 + chunk * 16;
            unsigned keep;
            asm volatile("s_mov_b32 %0, m0\n\ts_mov_b32 m0, %2\n\ts_nop 0\n\tglobal_load_lds_dwordx4 %1, off\n\ts_mov_b32 m0, %0"
                         : "=&s"(keep) : "v"(src), "s"(lds0 + (unsigned)p * 1024u) : "memory");
        }
        if (tid < 8) *reinterpret_cast<u32x4*>(smem + LDS_ZROW + tid * 16) = (u32x4){0u, 0u, 0u, 0u};
    }
    __builtin_amdgcn_sched_barrier(0);
#pragma unroll
    for (int jj = 0; jj < 8; ++jj)
        braw[2][jj] = *reinterpret_cast<const u32x2*>(w1lane + 2 * 4096 + jj * 512);
    unsigned warm;
    {
        const int id = mt * 4 + w;
        int row = (id < 48) ? kh * 3072 + id * 64 + lane : 6144 + kh * 128 + (id & 1) * 64 + lane;
        warm = *reinterpret_cast<const unsigned*>((const char*)w1 + (size_t)row * 512 + nt * 128);
    }
    __builtin_amdgcn_sched_barrier(0);

    u32x2 b1p = b1v, w2p = w2v; float b2p = b2v;
    asm volatile("" : "+v"(b1p), "+v"(w2p), "+v"(b2p));
    unsigned addr[8][4];
#pragma unroll
    for (int t = 0; t < 8; ++t) {
#pragma unroll
        for (int fi = 0; fi < 4; ++fi) {
            const unsigned v = (unsigned)(fi < 3 ? iv[t][fi] : iv3[t]);
            unsigned a = v * 128u + (((v >> 1) & 7u) << 4);
            if (fi == 3) a ^= (unsigned)(U & 7) << 4;
            asm volatile("" : "+v"(a));
            addr[t][fi] = a;
        }
    }
#pragma unroll
    for (int k = 0; k < 7; ++k) {
        f32x4 cfv;
#pragma unroll
        for (int r = 0; r < 4; ++r) {
            const float a = bf2f(wa[k][r]);
            const unsigned tw = (r & 2) ? wt[k][1] : wt[k][0];
            const float b = (r & 1) ? __uint_as_float(tw & 0xffff0000u) : __uint_as_float(tw << 16);
            cfv[r] = wok[k] ? (wdiag[k] ? a : a + b) : 0.0f;
        }
        *reinterpret_cast<f32x4*>(smem + LDS_SPARE + (w + 4 * k) * 1024 + lane * 16) = cfv;
    }
    __builtin_amdgcn_sched_barrier(0);
    asm volatile("s_waitcnt vmcnt(9) lgkmcnt(0)\n\ts_barrier" ::: "memory");
    __builtin_amdgcn_sched_barrier(0);

    f32x4 acc[8][4];
#pragma unroll
    for (int t = 0; t < 8; ++t)
#pragma unroll
        for (int u = 0; u < 4; ++u) acc[t][u] = (f32x4){0.f, 0.f, 0.f, 0.f};

    bf16x8 af[8];
#pragma unroll
    for (int t = 0; t < 8; ++t) af[t] = lds_read128(smem, addr[t][0]);
    const unsigned long long mynonce = *reinterpret_cast<const unsigned long long*>(smem + LDS_NONCE);
    const int prow0 = mt * 128 + kh * 64 + w * 16;
    unsigned praw[4][7], paddr[4][7];
    u32x4 bc[4], bn[4];
#pragma unroll
    for (int p = 0; p < 4; ++p) {
        const u32x2 a = braw[0][2 * p], b = braw[0][2 * p + 1];
        bc[0][p] = __builtin_amdgcn_perm(b[0], a[0], 0x05040100u);
        bc[1][p] = __builtin_amdgcn_perm(b[0], a[0], 0x07060302u);
        bc[2][p] = __builtin_amdgcn_perm(b[1], a[1], 0x05040100u);
        bc[3][p] = __builtin_amdgcn_perm(b[1], a[1], 0x07060302u);
    }
#pragma unroll
    for (int jj = 0; jj < 8; ++jj)
        braw[0][jj] = *reinterpret_cast<const u32x2*>(w1lane + 3 * 4096 + jj * 512);
    const int pk = lane & 3;
    const int peer = bid ^ (pk == 0 ? 1 : 2 * pk);
    GAS unsigned long long* preq = (GAS unsigned long long*)(ws + WS_REQ) + peer * 8;
    GAS unsigned long long* phel = (GAS unsigned long long*)(ws + WS_HEL) + peer * 8 + pk;
    GAS unsigned long long* mhel = (GAS unsigned long long*)(ws + WS_HEL) + bid * 8 + pk;
    unsigned long long pn = 0, hh = 0;
    __builtin_amdgcn_sched_barrier(0);

#pragma unroll
    for (int s = 0; s < 25; ++s) {
#pragma unroll
        for (int t = 0; t < 8; ++t) {
#pragma unroll
            for (int u = 0; u < 4; ++u)
                acc[t][u] = __builtin_amdgcn_mfma_f32_16x16x32_bf16(af[t], __builtin_bit_cast(bf16x8, bc[u]), acc[t][u], 0, 0, 0);
            if (s + 1 < 24)
                af[t] = lds_read128(smem, addr[t][(s + 1) >> 3] ^ (unsigned)(((s + 1) & 7) << 4));
            else if (s + 1 == 24)
                af[t] = lds_read128(smem, addr[t][3]);
            if (s + 1 < 25) {
                const int p = t >> 1;
                const u32x2 a = braw[(s + 1) % 3][2 * p], b = braw[(s + 1) % 3][2 * p + 1];
                if ((t & 1) == 0) {
                    bn[0][p] = __builtin_amdgcn_perm(b[0], a[0], 0x05040100u);
                    bn[1][p] = __builtin_amdgcn_perm(b[0], a[0], 0x07060302u);
                } else {
                    bn[2][p] = __builtin_amdgcn_perm(b[1], a[1], 0x05040100u);
                    bn[3][p] = __builtin_amdgcn_perm(b[1], a[1], 0x07060302u);
                    if (s + 4 < 24) {
                        braw[(s + 1) % 3][2 * p] = *reinterpret_cast<const u32x2*>(w1lane + (s + 4) * 4096 + (2 * p) * 512);
                        braw[(s + 1) % 3][2 * p + 1] = *reinterpret_cast<const u32x2*>(w1lane + (s + 4) * 4096 + (2 * p + 1) * 512);
                    } else if (s + 4 == 24) {
                        braw[(s + 1) % 3][2 * p] = *reinterpret_cast<const u32x2*>(w1last + (2 * p) * 512);
                        braw[(s + 1) % 3][2 * p + 1] = *reinterpret_cast<const u32x2*>(w1last + (2 * p + 1) * 512);
                    }
                }
            }
            if (s >= 2 && s < 6 && t < 7) {
                const int fld = (t < 6) ? 16 * t + c : (c < 4 ? 96 + c : 0);
                praw[s - 2][t] = (unsigned)idx[(prow0 + nt * 4 + (s - 2)) * NF + fld];
            }
            if (s >= 12 && s < 16 && t < 7) {
                unsigned v = praw[s - 12][t];
                if (t == 6) v = (c < 4) ? v : 1000u;
                unsigned a = v * 128u + (((unsigned)q ^ ((v >> 1) & 7u)) << 4);
                asm volatile("" : "+v"(a));
                paddr[s - 12][t] = a;
            }
            if (t == 7) {
                if (s == 8) {
                    unsigned d0 = __hip_atomic_load((GAS unsigned*)(ws + WS_X) + ((bid ^ pk) * 4 + w) * 1024, RLX_AGENT);
                    asm volatile("" :: "v"(d0));
                }
                if (s == 0) pn = __hip_atomic_load(preq, RLX_AGENT);
                if (s == 6) __hip_atomic_store(phel, pn, RLX_AGENT);
                if (s == 16) hh = __hip_atomic_load(mhel, RLX_AGENT);
                if (s == 20) { asm volatile("" : "+v"(preq) : "v"(hh)); pn = __hip_atomic_load(preq, RLX_AGENT); }
                if (s == 24) __hip_atomic_store(phel, pn, RLX_AGENT);
            }
            __builtin_amdgcn_sched_group_barrier(0x008, 1, 0);
            __builtin_amdgcn_sched_group_barrier(0x002, 1, 0);
            __builtin_amdgcn_sched_group_barrier(0x020, 1, 0);
            __builtin_amdgcn_sched_group_barrier(0x008, 1, 0);
            __builtin_amdgcn_sched_group_barrier(0x002, 1, 0);
            __builtin_amdgcn_sched_group_barrier(0x020, 1, 0);
            __builtin_amdgcn_sched_group_barrier(0x008, 1, 0);
            __builtin_amdgcn_sched_group_barrier(0x002, 2, 0);
            __builtin_amdgcn_sched_group_barrier(0x008, 1, 0);
            __builtin_amdgcn_sched_group_barrier(0x100, 1, 0);
            __builtin_amdgcn_sched_group_barrier(0x002, 1, 0);
        }
        if (s + 1 < 25) {
#pragma unroll
            for (int u = 0; u < 4; ++u) { asm volatile("" : "+v"(bn[u])); bc[u] = bn[u]; }
        }
        if (s == 3) asm volatile("" :: "v"(warm));
        __builtin_amdgcn_sched_barrier(0);
    }
    asm volatile("" ::: "memory");
    if (!__all(hh == mynonce)) {
        unsigned spins = 0;
        for (;;) {
            pn = __hip_atomic_load(preq, RLX_AGENT);
            __hip_atomic_store(phel, pn, RLX_AGENT);
            hh = __hip_atomic_load(mhel, RLX_AGENT);
            if (__all(hh == mynonce)) break;
            __builtin_amdgcn_s_sleep(8);
            if (++spins > (1u << 22)) break;
        }
        asm volatile("" : "+v"(preq) : "v"(hh));
        pn = __hip_atomic_load(preq, RLX_AGENT);
        __hip_atomic_store(phel, pn, RLX_AGENT);
    }
    f32x4 cf[28];
#pragma unroll
    for (int t = 0; t < 28; ++t)
        cf[t] = *reinterpret_cast<const f32x4*>(smem + LDS_SPARE + t * 1024 + lane * 16);
#pragma unroll
    for (int t = 0; t < 28; ++t) asm volatile("" : "+v"(cf[t]));
    __syncthreads();
#pragma unroll
    for (int r = 1; r < 4; ++r) {
        char* dst = smem + LDS_SPARE + (((w + r) & 3) * 8192) + lane * 16;
#pragma unroll
        for (int h = 0; h < 2; ++h)
#pragma unroll
            for (int u = 0; u < 4; ++u)
                *reinterpret_cast<f32x4*>(dst + (h * 4 + u) * 1024) = acc[r * 2 + h][u];
        __syncthreads();
        const char* src = smem + LDS_SPARE + w * 8192 + lane * 16;
#pragma unroll
        for (int h = 0; h < 2; ++h)
#pragma unroll
            for (int u = 0; u < 4; ++u)
                acc[h][u] += *reinterpret_cast<const f32x4*>(src + (h * 4 + u) * 1024);
        if (r < 3) __syncthreads();
    }
    const __amdgpu_buffer_rsrc_t xr = __builtin_amdgcn_make_buffer_rsrc(ws + WS_X, 0, 1024 * 4096, 0x00020000);
    {
        const unsigned off = (unsigned)((bid * 4 + w) * 4096 + lane * 16);
#pragma unroll
        for (int u = 0; u < 4; ++u)
            __builtin_amdgcn_raw_buffer_store_b128(__builtin_bit_cast(u32x4, acc[1][u]), xr, off + u * 1024, 0, 16);
    }
    float lp[4];
    GAS unsigned long long* gr = (GAS unsigned long long*)(ws + WS_GRAN) + ((mt * 2 + kh) * 4 + w) * 128;
    bf16x8 e[2][7][2];
#pragma unroll
    for (int I = 0; I < 7; ++I) {
        e[0][I][0] = lds_read128(smem, paddr[0][I]);
        e[0][I][1] = lds_read128(smem, paddr[0][I] ^ 64u);
    }
#pragma unroll
    for (int i = 0; i < 4; ++i) {
#pragma unroll
        for (int I = 0; I < 7; ++I) { asm volatile("" : "+a"(e[i & 1][I][0])); asm volatile("" : "+a"(e[i & 1][I][1])); }
        if (i < 3) {
#pragma unroll
            for (int I = 0; I < 7; ++I) {
                e[(i + 1) & 1][I][0] = lds_read128(smem, paddr[i + 1][I]);
                e[(i + 1) & 1][I][1] = lds_read128(smem, paddr[i + 1][I] ^ 64u);
            }
        }
        if (i == 2) {
            asm volatile("s_waitcnt vmcnt(0)" ::: "memory");
            __syncthreads();
            if (tid == 0)
                __hip_atomic_store((GAS unsigned long long*)(ws + WS_TAG) + (bid ^ 1) * 8, pn, RLX_AGENT);
        }
        if (i == 3) {
            GAS unsigned long long* fl = (GAS unsigned long long*)(ws + WS_TAG) + bid * 8;
            unsigned spins = 0;
            while (!__all(__hip_atomic_load(fl, RLX_AGENT) == mynonce)) {
                __builtin_amdgcn_s_sleep(4);
                if (++spins > (1u << 22)) break;
            }
            __builtin_amdgcn_fence(__ATOMIC_ACQUIRE, "wavefront");
            const unsigned off = (unsigned)(((bid ^ 1) * 4 + w) * 4096 + lane * 16);
#pragma unroll
            for (int u = 0; u < 4; ++u)
                acc[0][u] += __builtin_bit_cast(f32x4, __builtin_amdgcn_raw_buffer_load_b128(xr, off + u * 1024, 0, 16));
            const float bb[4] = {__uint_as_float(b1p[0] << 16), __uint_as_float(b1p[0] & 0xffff0000u),
                                 __uint_as_float(b1p[1] << 16), __uint_as_float(b1p[1] & 0xffff0000u)};
            const float ww[4] = {__uint_as_float(w2p[0] << 16), __uint_as_float(w2p[0] & 0xffff0000u),
                                 __uint_as_float(w2p[1] << 16), __uint_as_float(w2p[1] & 0xffff0000u)};
            float rs[4];
#pragma unroll
            for (int r = 0; r < 4; ++r) {
                float a = 0.f;
#pragma unroll
                for (int u = 0; u < 4; ++u) {
                    float y = bfround(acc[0][u][r]);
                    y = bfround(y + bb[u]);
                    const float hh = bfround(gelu_tanh(y));
                    a = fmaf(hh, ww[u], a);
                }
                rs[r] = a;
            }
#pragma unroll
            for (int m = 1; m < 16; m <<= 1)
#pragma unroll
                for (int r = 0; r < 4; ++r) rs[r] += __shfl_xor(rs[r], m, 64);
            const int xq = nt ^ q;
            unsigned tlo = (unsigned)__shfl((int)(unsigned)pn, xq, 64), thi = (unsigned)__shfl((int)(unsigned)(pn >> 32), xq, 64);
            if (xq == 0) { tlo = (unsigned)mynonce; thi = (unsigned)(mynonce >> 32); }
            if (c == 0) {
#pragma unroll
                for (int r = 0; r < 4; ++r) {
                    const unsigned long long vb = (unsigned long long)__float_as_uint(rs[r]);
                    __hip_atomic_store(gr + (nt * 16 + q * 4 + r) * 2, ((unsigned long long)tlo << 32) | vb, RLX_AGENT);
                    __hip_atomic_store(gr + (nt * 16 + q * 4 + r) * 2 + 1, ((unsigned long long)thi << 32) | vb, RLX_AGENT);
                }
            }
        }
        __builtin_amdgcn_sched_barrier(0);
        float s0 = 0.f, s1 = 0.f, s2 = 0.f, s3 = 0.f;
        f32x4 d[3];
        int t = 0;
#pragma unroll
        for (int I = 0; I < 7; ++I) {
#pragma unroll
            for (int J = I; J < 7; ++J, ++t) {
                if (t < 2) {
                    asm volatile(
                        "s_nop 1\n\t"
                        "v_mfma_f32_16x16x32_bf16 %0, %1, %2, 0\n\t"
                        "v_mfma_f32_16x16x32_bf16 %0, %3, %4, %0"
                        : "=&v"(d[t % 3])
                        : "a"(e[i & 1][I][0]), "a"(e[i & 1][J][0]), "a"(e[i & 1][I][1]), "a"(e[i & 1][J][1]));
                } else {
                    const f32x4 dp = d[(t + 1) % 3];
                    const f32x4 cp = cf[t - 2];
                    if (t == 2) {
                        asm volatile(
                            "v_mfma_f32_16x16x32_bf16 %0, %5, %6, 0\n\t"
                            "s_nop 4\n\t"
                            "v_fmac_f32 %1, %9, %13\n\t"
                            "v_fmac_f32 %2, %10, %14\n\t"
                            "v_mfma_f32_16x16x32_bf16 %0, %7, %8, %0\n\t"
                            "v_fmac_f32 %3, %11, %15\n\t"
                            "v_fmac_f32 %4, %12, %16"
                            : "=&v"(d[t % 3]), "+v"(s0), "+v"(s1), "+v"(s2), "+v"(s3)
                            : "a"(e[i & 1][I][0]), "a"(e[i & 1][J][0]), "a"(e[i & 1][I][1]), "a"(e[i & 1][J][1]),
                              "v"(cp[0]), "v"(cp[1]), "v"(cp[2]), "v"(cp[3]),
                              "v"(dp[0]), "v"(dp[1]), "v"(dp[2]), "v"(dp[3]));
                    } else {
                        asm volatile(
                            "v_mfma_f32_16x16x32_bf16 %0, %5, %6, 0\n\t"
                            "v_fmac_f32 %1, %9, %13\n\t"
                            "v_fmac_f32 %2, %10, %14\n\t"
                            "v_mfma_f32_16x16x32_bf16 %0, %7, %8, %0\n\t"
                            "v_fmac_f32 %3, %11, %15\n\t"
                            "v_fmac_f32 %4, %12, %16"
                            : "=&v"(d[t % 3]), "+v"(s0), "+v"(s1), "+v"(s2), "+v"(s3)
                            : "a"(e[i & 1][I][0]), "a"(e[i & 1][J][0]), "a"(e[i & 1][I][1]), "a"(e[i & 1][J][1]),
                              "v"(cp[0]), "v"(cp[1]), "v"(cp[2]), "v"(cp[3]),
                              "v"(dp[0]), "v"(dp[1]), "v"(dp[2]), "v"(dp[3]));
                    }
                }
            }
        }
        {
            const f32x4 da = d[26 % 3], db = d[27 % 3], ca = cf[26], cb = cf[27];
            asm volatile(
                "s_nop 1\n\t"
                "v_fmac_f32 %0, %4, %8\n\t"
                "v_fmac_f32 %1, %5, %9\n\t"
                "v_fmac_f32 %2, %6, %10\n\t"
                "v_fmac_f32 %3, %7, %11\n\t"
                "v_fmac_f32 %0, %12, %16\n\t"
                "v_fmac_f32 %1, %13, %17\n\t"
                "v_fmac_f32 %2, %14, %18\n\t"
                "v_fmac_f32 %3, %15, %19"
                : "+v"(s0), "+v"(s1), "+v"(s2), "+v"(s3)
                : "v"(ca[0]), "v"(ca[1]), "v"(ca[2]), "v"(ca[3]), "v"(da[0]), "v"(da[1]), "v"(da[2]), "v"(da[3]),
                  "v"(cb[0]), "v"(cb[1]), "v"(cb[2]), "v"(cb[3]), "v"(db[0]), "v"(db[1]), "v"(db[2]), "v"(db[3]));
        }
        lp[i] = (s0 + s1) + (s2 + s3);
        __builtin_amdgcn_sched_barrier(0);
    }
#pragma unroll
    for (int m = 1; m < 64; m <<= 1)
#pragma unroll
        for (int i = 0; i < 4; ++i) lp[i] += __shfl_xor(lp[i], m, 64);

    {
        float v = 0.f;
        unsigned spins = 0;
        for (;;) {
            bool ok = true;
            if (lane < 16) {
                const unsigned long long xa = __hip_atomic_load(gr + ((lane >> 2) * 16 + nt * 4 + (lane & 3)) * 2, RLX_AGENT);
                const unsigned long long xb = __hip_atomic_load(gr + ((lane >> 2) * 16 + nt * 4 + (lane & 3)) * 2 + 1, RLX_AGENT);
                v = __uint_as_float((unsigned)xa);
                ok = (xa >> 32) == (mynonce & 0xffffffffull) && (xb >> 32) == (mynonce >> 32) && (unsigned)xa == (unsigned)xb;
            }
            if (__all(ok)) break;
            __builtin_amdgcn_s_sleep(8);
            if (++spins > (1u << 22)) break;
        }
        v += __shfl_xor(v, 4, 64);
        v += __shfl_xor(v, 8, 64);
        const float lpv = (lane & 2) ? ((lane & 1) ? lp[3] : lp[2]) : ((lane & 1) ? lp[1] : lp[0]);
        if (lane < 4) out[prow0 + nt * 4 + lane] = __float2bfloat16(v + b2p + lpv);
    }
}

extern "C" void kernel_launch(void* const* d_in, const int* in_sizes, int n_in,
                              void* d_out, int out_size, void* d_ws, size_t ws_size,
                              hipStream_t stream) {
    const int* idx = (const int*)d_in[0];
    const unsigned short* table = (const unsigned short*)d_in[1];
    const unsigned short* wp = (const unsigned short*)d_in[2];
    const unsigned short* w1 = (const unsigned short*)d_in[3];
    const unsigned short* b1 = (const unsigned short*)d_in[4];
    const unsigned short* w2 = (const unsigned short*)d_in[5];
    const unsigned short* b2 = (const unsigned short*)d_in[6];
    dcp_kernel<<<256, 256, 0, stream>>>(idx, table, wp, w1, b1, w2, b2, (__hip_bfloat16*)d_out, (char*)d_ws);
}
```
